```python
import torch

def _softmax(x, axis=-1):
    return torch.softmax(x, dim=axis)
def _gelu(x):
    return torch.nn.functional.gelu(x, approximate="tanh")
def _silu(x):
    return torch.nn.functional.silu(x)
def _sigmoid(x):
    return torch.sigmoid(x)
def _layernorm(x, axis=-1, eps=1e-5):
    m = x.mean(dim=axis, keepdim=True)
    v = x.var(dim=axis, keepdim=True, unbiased=False)
    return (x - m) / torch.sqrt(v + eps)
def _scatter_add(tgt, idx, src, axis=0):
    return tgt.clone().index_add_(axis, idx, src)
def _segment_sum(src, seg, n):
    out = torch.zeros((n,) + src.shape[1:], dtype=src.dtype)
    return out.index_add_(0, seg, src)
def _pad(x, pw):
    flat = tuple(v for pair in reversed(pw) for v in pair)
    return torch.nn.functional.pad(x, flat)

kernel_name = 'recurrent_scan__softmax_full_mha__dense_activation_squared_relu__prompt'

def setup_inputs():

    _w = (lambda a: (lambda t: t.to(torch.bfloat16) if t.is_floating_point() else t)(torch.as_tensor(a)))
    def _A_setup():
        B, S, D = 1, 2048, 576
        return {
            "x": _w((lambda *s: torch.randn(*s, dtype=torch.bfloat16) * 0.05)(B, S, D)),
            "wqkv": _w((lambda *s: torch.randn(*s, dtype=torch.bfloat16) * 0.05)(D, 3 * D)),
            "wo": _w((lambda *s: torch.randn(*s, dtype=torch.bfloat16) * 0.05)(D, D)),
        }
    _A_in = _A_setup()

    def _B_setup():
        B, S, D, F = 1, 2048, 576, 4608
        return {
            "x": _w((lambda *s: torch.randn(*s, dtype=torch.bfloat16) * 0.05)(B, S, D)),
            "w1": _w((lambda *s: torch.randn(*s, dtype=torch.bfloat16) * 0.05)(D, F)),
            "b1": _w((lambda *s: torch.randn(*s, dtype=torch.bfloat16) * 0.4)(F)),
            "w2": _w((lambda *s: torch.randn(*s, dtype=torch.bfloat16) * 0.05)(F, D)),
            "b2": _w((lambda *s: torch.randn(*s, dtype=torch.bfloat16) * 3.2)(D)),
        }
    _B_in = _B_setup()

    return {
        "A_x": _A_in["x"],
        "A_wqkv": _A_in["wqkv"],
        "A_wo": _A_in["wo"],
        "B_x": _B_in["x"],
        "B_w1": _B_in["w1"],
        "B_b1": _B_in["b1"],
        "B_w2": _B_in["w2"],
        "B_b2": _B_in["b2"],
    }

def reference(A_x, A_wqkv, A_wo, B_x, B_w1, B_b1, B_w2, B_b2):
    def _A_ref(x, wqkv, wo):
        B, S, D = x.shape
        H = 32; Dh = D // H
        qkv = (lambda x, *d: x.permute(*d))((x @ wqkv).reshape(B, S, 3, H, Dh), 2, 0, 3, 1, 4)
        q, k, v = qkv[0], qkv[1], qkv[2]
        scale = Dh ** -0.5
        scores = torch.einsum("bhqd,bhkd->bhqk", q, k) * scale
        p = _softmax(scores, axis=-1)
        o = torch.einsum("bhqk,bhkd->bhqd", p, v)
        return (lambda x, *d: x.permute(*d))(o, 0, 2, 1, 3).reshape(B, S, D) @ wo

    def _B_ref(x, w1, b1, w2, b2):
        h = torch.einsum("bsd,df->bsf", x, w1) + b1
        zero = torch.zeros_like(h)
        r = torch.where(h > zero, h, zero)
        h = r * r
        out = torch.einsum("bsf,fd->bsd", h, w2) + b2
        return out

    x = A_x
    xn = _layernorm(x, axis=-1)
    s = _A_ref(xn, A_wqkv, A_wo)
    g = _silu(_B_ref(xn, B_w1, B_b1, B_w2, B_b2))
    return x + s * g
```

```cpp
#include <hip/hip_runtime.h>
#include <stdint.h>

#define DI __device__ __forceinline__
#define LAS __attribute__((address_space(3)))
typedef short bf16x8 __attribute__((ext_vector_type(8)));
typedef short s16x4 __attribute__((ext_vector_type(4)));
typedef float f32x2 __attribute__((ext_vector_type(2)));
typedef float f32x4 __attribute__((ext_vector_type(4)));
typedef float f32x16 __attribute__((ext_vector_type(16)));
typedef unsigned u32x2 __attribute__((ext_vector_type(2)));
typedef unsigned u32x4 __attribute__((ext_vector_type(4)));
typedef __bf16 bf16x2_t __attribute__((ext_vector_type(2)));
typedef unsigned short u16;

constexpr int SEQ = 2048, DM = 576, FF = 4608, NHEAD = 32;
constexpr size_t XN_OFF = 0;
constexpr size_t QF_OFF = XN_OFF + 2359296;
constexpr size_t KF_OFF = QF_OFF + 2359296;
constexpr size_t VT_OFF = KF_OFF + 2359296;
constexpr size_t HF_OFF = VT_OFF + 2359296;
constexpr size_t OF_OFF = HF_OFF + 18874368;
constexpr size_t GP_OFF = OF_OFF + 4194304;
constexpr size_t WS_END = GP_OFF + 18874368;

DI unsigned cvtpk(float lo, float hi) { f32x2 v = {lo, hi}; bf16x2_t b = __builtin_convertvector(v, bf16x2_t); return __builtin_bit_cast(unsigned, b); }
DI float bf_lo(unsigned w) { return __uint_as_float(w << 16); }
DI float bf_hi(unsigned w) { return __uint_as_float(w & 0xffff0000u); }
DI float rbf(float x) { return bf_lo(cvtpk(x, x)); }
template <int V> struct IC { static constexpr int value = V; };
template <int B, int E, typename Fn> DI void static_for(Fn&& fn) { if constexpr (B < E) { fn(IC<B>{}); static_for<B + 1, E>(fn); } }
#define SBAR() __builtin_amdgcn_sched_barrier(0)
#define MFMA16(a, b, c) __builtin_amdgcn_mfma_f32_16x16x32_bf16((a), (b), (c), 0, 0, 0)
#define MFMA32(a, b, c) __builtin_amdgcn_mfma_f32_32x32x16_bf16((a), (b), (c), 0, 0, 0)

DI void glds_v(const void* g, unsigned lds_base) {
    unsigned sv; asm volatile("s_mov_b32 %0, m0\n\ts_mov_b32 m0, %2\n\ts_nop 0\n\tglobal_load_lds_dwordx4 %1, off\n\ts_mov_b32 m0, %0" : "=&s"(sv) : "v"(g), "s"(lds_base) : "memory"); }
DI void glds_s(unsigned voff, const void* sbase, unsigned lds_base) {
    unsigned sv; asm volatile("s_mov_b32 %0, m0\n\ts_mov_b32 m0, %3\n\ts_nop 0\n\tglobal_load_lds_dwordx4 %1, %2\n\ts_mov_b32 m0, %0" : "=&s"(sv) : "v"(voff), "s"(sbase), "s"(lds_base) : "memory"); }
DI void gload16(bf16x8& d, unsigned voff, const void* sbase) { asm volatile("global_load_dwordx4 %0, %1, %2" : "=v"(d) : "v"(voff), "s"(sbase) : "memory"); }
DI void gstore16(unsigned voff, void* sbase, u32x4 v) { asm volatile("global_store_dwordx4 %0, %1, %2\n\ts_nop 1" :: "v"(voff), "v"(v), "s"(sbase) : "memory"); }
template <int N> DI void wait_vm() { asm volatile("s_waitcnt vmcnt(%0)" :: "n"(N) : "memory"); }
template <int N> DI void wait_vm_bar() { asm volatile("s_waitcnt vmcnt(%0) lgkmcnt(0)\n\ts_barrier" :: "n"(N) : "memory"); }
typedef short v4i16_t __attribute__((ext_vector_type(4)));
typedef LAS const char* lds_cptr;
DI s16x4 vtr(lds_cptr p) { return __builtin_bit_cast(s16x4, __builtin_amdgcn_ds_read_tr16_b64_v4i16((LAS v4i16_t*)p)); }
DI bf16x8 trfrag(lds_cptr p, int off2) { s16x4 lo = vtr(p), hi = vtr(p + off2); return __builtin_shufflevector(lo, hi, 0, 1, 2, 3, 4, 5, 6, 7); }
DI bf16x8 ldsfrag(lds_cptr p) { return *(const LAS bf16x8*)p; }
template <typename T> DI T* uni(T* p) {
    uint64_t a = (uint64_t)p; unsigned lo = __builtin_amdgcn_readfirstlane((unsigned)a), hi = __builtin_amdgcn_readfirstlane((unsigned)(a >> 32));
    return (T*)(((uint64_t)hi << 32) | lo); }

DI void ln_rows(const u16* __restrict__ x, char* ws, int row0, int lane) {
    const int r = lane >> 3, c = lane & 7, row = row0 + r;
    const u16* xr = x + (size_t)row * DM;
    u32x2 a[9], b[9];
#pragma unroll
    for (int i = 0; i < 9; ++i) { const int oc = c + 8 * i, s = oc >> 2, g = oc & 3;
        a[i] = *(const u32x2*)(xr + 32 * s + 4 * g); b[i] = *(const u32x2*)(xr + 32 * s + 16 + 4 * g); }
    float sum = 0.f;
#pragma unroll
    for (int i = 0; i < 9; ++i) sum += (bf_lo(a[i][0]) + bf_hi(a[i][0])) + (bf_lo(a[i][1]) + bf_hi(a[i][1])) + (bf_lo(b[i][0]) + bf_hi(b[i][0])) + (bf_lo(b[i][1]) + bf_hi(b[i][1]));
    sum += __shfl_xor(sum, 1); sum += __shfl_xor(sum, 2); sum += __shfl_xor(sum, 4);
    const float mean = sum * (1.0f / DM);
    float sq = 0.f;
#pragma unroll
    for (int i = 0; i < 9; ++i) {
#pragma unroll
        for (int k = 0; k < 2; ++k) { float d0 = bf_lo(a[i][k]) - mean, d1 = bf_hi(a[i][k]) - mean, d2 = bf_lo(b[i][k]) - mean, d3 = bf_hi(b[i][k]) - mean; sq += (d0 * d0 + d1 * d1) + (d2 * d2 + d3 * d3); } }
    sq += __shfl_xor(sq, 1); sq += __shfl_xor(sq, 2); sq += __shfl_xor(sq, 4);
    const float m_bf = rbf(mean), v_bf = rbf(sq * (1.0f / DM));
    const float den = rbf(sqrtf(rbf(v_bf + 1e-5f)));
    const float inv = 1.0f / den;
    char* dst = ws + XN_OFF + (size_t)(row >> 4) * 18 * 1024 + (row & 15) * 16;
#pragma unroll
    for (int i = 0; i < 9; ++i) { const int oc = c + 8 * i, s = oc >> 2, g = oc & 3;
        u32x4 o;
#pragma unroll
        for (int k = 0; k < 2; ++k) {
            o[k]     = cvtpk(rbf(bf_lo(a[i][k]) - m_bf) * inv, rbf(bf_hi(a[i][k]) - m_bf) * inv);
            o[2 + k] = cvtpk(rbf(bf_lo(b[i][k]) - m_bf) * inv, rbf(bf_hi(b[i][k]) - m_bf) * inv); }
        *(u32x4*)(dst + s * 1024 + g * 256) = o; }
}
__global__ __launch_bounds__(64) void k_ln(const u16* __restrict__ x, char* ws) { ln_rows(x, ws, blockIdx.x * 8, threadIdx.x); }

constexpr int GA_ROW = 224, GA_STAGE = 64 * GA_ROW, GA_R = 4, GA_BIAS = GA_R * GA_STAGE;
constexpr float QSCALE = 0.23570226039551584f * 1.4426950408889634f;

template <int NQ, int NF1, bool ISV>
DI void gemmA_body(const u16* __restrict__ wqkv, const u16* __restrict__ w1, const u16* __restrict__ b1, char* ws, char* lds, int rb, int tq0, int tf0) {
    constexpr int NT1 = NQ + NF1, NT2 = 6, LAG = 3;
    const int tid = threadIdx.x, lane = tid & 63, wid = __builtin_amdgcn_readfirstlane(tid >> 6);
    const unsigned lds0 = (unsigned)(uintptr_t)lds;
    const int T = rb * 8 + wid;
    const char* src[2]; unsigned inc[2]; bool val1[2], val2[2]; const char* src2[2];
#pragma unroll
    for (int i = 0; i < 2; ++i) { const int ci = 112 * wid + 64 * i + lane, row = ci / 14, c = ci % 14, unit = c >> 1, half = c & 1;
        const bool inr = (i == 0) || (lane < 48);
        if (unit < NQ) { src[i] = (const char*)wqkv + (size_t)row * 3456 + (tq0 + unit) * 32 + half * 16; inc[i] = 64 * 3456; }
        else { src[i] = (const char*)w1 + (size_t)row * 9216 + (tf0 + unit - NQ) * 32 + half * 16; inc[i] = 64 * 9216; }
        val1[i] = inr && unit < NT1; val2[i] = inr && unit < NT2;
        src2[i] = (const char*)w1 + (size_t)row * 9216 + (tf0 + NF1 + unit) * 32 + half * 16; }
    const unsigned sdst = lds0 + wid * 112 * 16;
    auto stage_issue = [&](auto TT) { constexpr int t = TT.value;
        const unsigned d = sdst + (t % GA_R) * GA_STAGE;
        if constexpr (t < 9) {
#pragma unroll
            for (int i = 0; i < 2; ++i) { if (val1[i]) glds_v(src[i], __builtin_amdgcn_readfirstlane(d + i * 1024)); src[i] += inc[i]; }
        } else {
#pragma unroll
            for (int i = 0; i < 2; ++i) { if (val2[i]) glds_v(src2[i], __builtin_amdgcn_readfirstlane(d + i * 1024)); src2[i] += 64 * 9216; }
        } };
    bf16x8 xf[18][2];
    const char* xbase = uni(ws + XN_OFF + (size_t)(2 * T) * 18 * 1024);
    const unsigned xoff = lane * 16;
    auto x_issue = [&](auto TT) { constexpr int t = TT.value;
#pragma unroll
        for (int k = 0; k < 2; ++k) { gload16(xf[2 * t + k][0], xoff, xbase + (2 * t + k) * 1024); gload16(xf[2 * t + k][1], xoff, xbase + (18 + 2 * t + k) * 1024); } };
    if (tid < 160) { const int f = tf0 * 16 + tid; ((float*)(lds + GA_BIAS))[tid] = f < FF ? __uint_as_float((unsigned)b1[f] << 16) : 0.f; }
    asm volatile("s_waitcnt vmcnt(0) lgkmcnt(0)" ::: "memory");
    static_for<0, 3>([&](auto TT) { stage_issue(TT); x_issue(TT); });
    const lds_cptr wp = (lds_cptr)lds + ((lane >> 4) * 4 + ((lane & 15) >> 2)) * GA_ROW + (lane & 3) * 8;
    const lds_cptr bp = (lds_cptr)lds + GA_BIAS + (lane >> 4) * 16;
    const f32x4 zero4 = {0.f, 0.f, 0.f, 0.f};
    f32x4 acc[7][2];
    bf16x8 wf[LAG + 1];
    constexpr int N1 = 18 * NT1, N2 = 18 * NT2, NALL = N1 + N2;
    auto frag_ptr = [&](auto I) { constexpr int i = I.value, s = i < N1 ? i / NT1 : (i - N1) / NT2, n = i < N1 ? i % NT1 : (i - N1) % NT2, t = i < N1 ? i / (2 * NT1) : 9 + (i - N1) / (2 * NT2);
        return wp + (t % GA_R) * GA_STAGE + (s & 1) * 32 * GA_ROW + n * 32; };
    auto sync_stage = [&](auto TT) { constexpr int t = TT.value;
        constexpr int N = t <= 6 ? 12 : t == 7 ? 8 : t <= 9 ? 4 : t <= 12 ? 4 + NT1 : t <= 15 ? 4 : t == 16 ? 2 : 0;
        wait_vm_bar<N>();
        if constexpr (t <= 8) asm volatile("" : "+v"(xf[2 * t][0]), "+v"(xf[2 * t][1]), "+v"(xf[2 * t + 1][0]), "+v"(xf[2 * t + 1][1]));
        if constexpr (t + 3 < 18) { stage_issue(IC<t + 3>{}); if constexpr (t + 3 < 9) x_issue(IC<t + 3>{}); }
    };
    auto epi_h = [&](int n, int tf) {
#pragma unroll
        for (int u = 0; u < 2; ++u) { u32x4 o;
#pragma unroll
            for (int k = 0; k < 2; ++k) { float a0 = fmaxf(acc[n][u][2 * k], 0.f), a1 = fmaxf(acc[n][u][2 * k + 1], 0.f), b0 = fmaxf(acc[n + 1][u][2 * k], 0.f), b1_ = fmaxf(acc[n + 1][u][2 * k + 1], 0.f);
                o[k] = cvtpk(a0 * a0, a1 * a1); o[2 + k] = cvtpk(b0 * b0, b1_ * b1_); }
            gstore16(xoff, uni(ws + HF_OFF + ((size_t)(2 * T + u) * 144 + (tf >> 1)) * 1024), o); } };
    auto epi_qk = [&](int n, int tq) {
        const float sc = tq < 36 ? QSCALE : 1.0f;
        unsigned p0x = cvtpk(acc[n][0][0] * sc, acc[n][0][1] * sc), p0y = cvtpk(acc[n][0][2] * sc, acc[n][0][3] * sc);
        unsigned p1x = cvtpk(acc[n][1][0] * sc, acc[n][1][1] * sc), p1y = cvtpk(acc[n][1][2] * sc, acc[n][1][3] * sc);
        auto sx = __builtin_amdgcn_permlane16_swap(p0x, p1x, false, false); auto sy = __builtin_amdgcn_permlane16_swap(p0y, p1y, false, false);
        u32x4 o = {sx[0], sy[0], sx[1], sy[1]};
        gstore16(xoff, uni(ws + QF_OFF + ((size_t)tq * 64 + T) * 1024), o); };
    auto epi_v = [&](int n, int tq) {
        unsigned p0x = cvtpk(acc[n][0][0], acc[n][0][1]), p0y = cvtpk(acc[n][0][2], acc[n][0][3]);
        unsigned p1x = cvtpk(acc[n][1][0], acc[n][1][1]), p1y = cvtpk(acc[n][1][2], acc[n][1][3]);
        auto sx = __builtin_amdgcn_permlane32_swap(p0x, p1x, false, false); auto sy = __builtin_amdgcn_permlane32_swap(p0y, p1y, false, false);
        u32x4 o = {sx[0], sy[0], sx[1], sy[1]};
        const int col = (tq - 72) * 16 + (lane & 15), hd = col / 18, r = col - hd * 18, kg = 2 * T + (lane >> 5), h = (lane >> 4) & 1;
        gstore16((unsigned)((((hd * 128 + kg) * 2 + h) * 18 + r) * 16), uni(ws + VT_OFF), o); };

    sync_stage(IC<0>{});
    static_for<0, LAG>([&](auto I) { wf[I.value] = trfrag(frag_ptr(I), 16 * GA_ROW); });
    static_for<0, NALL>([&](auto I) {
        constexpr int i = I.value, j = i + LAG;
        if constexpr (j < NALL) {
            constexpr bool st = j < N1 ? (j % (2 * NT1)) == 0 : ((j - N1) % (2 * NT2)) == 0;
            if constexpr (st) { SBAR(); sync_stage(IC<(j < N1 ? j / (2 * NT1) : 9 + (j - N1) / (2 * NT2))>{}); SBAR(); }
            wf[j % (LAG + 1)] = trfrag(frag_ptr(IC<j>{}), 16 * GA_ROW); }
        constexpr bool p1 = i < N1; constexpr int s = p1 ? i / NT1 : (i - N1) / NT2, n = p1 ? i % NT1 : (i - N1) % NT2;
        const bf16x8 w = wf[i % (LAG + 1)];
        constexpr bool isq = p1 && n < NQ;
        f32x4 c0, c1;
        if constexpr (s == 0) { if constexpr (isq) { c0 = zero4; c1 = zero4; } else { constexpr int bi = p1 ? n - NQ : NF1 + n; c0 = *(const LAS f32x4*)(bp + bi * 64); c1 = c0; } }
        else { c0 = acc[n][0]; c1 = acc[n][1]; }
        if constexpr (isq && ISV) { acc[n][0] = MFMA16(xf[s][0], w, c0); acc[n][1] = MFMA16(xf[s][1], w, c1); }
        else { acc[n][0] = MFMA16(w, xf[s][0], c0); acc[n][1] = MFMA16(w, xf[s][1], c1); }
        SBAR();
        if constexpr (i == N1 - 1) {
            static_for<0, NQ>([&](auto Q) { if constexpr (ISV) epi_v(Q.value, tq0 + Q.value); else epi_qk(Q.value, tq0 + Q.value); });
            static_for<0, NF1 / 2>([&](auto Q) { epi_h(NQ + 2 * Q.value, tf0 + 2 * Q.value); });
            SBAR();
        }
    });
    static_for<0, NT2 / 2>([&](auto Q) { epi_h(2 * Q.value, tf0 + NF1 + 2 * Q.value); });
}

__global__ __launch_bounds__(512, 2) void k_gemmA(const u16* __restrict__ wqkv, const u16* __restrict__ w1, const u16* __restrict__ b1, char* ws) {
    extern __shared__ __attribute__((aligned(16))) char lds[];
    const int b = blockIdx.x, xcd = b & 7, j = b >> 3, rb = j & 7, cg = xcd * 4 + (j >> 3);
    const int pr = cg >> 1, tf0 = pr * 18 + (cg & 1) * 10;
    if ((cg & 1) == 0) gemmA_body<3, 4, false>(wqkv, w1, b1, ws, lds, rb, pr * 3, tf0);
    else if (pr < 9)   gemmA_body<4, 2, true>(wqkv, w1, b1, ws, lds, rb, 72 + pr * 4, tf0);
    else if (pr < 12)  gemmA_body<4, 2, false>(wqkv, w1, b1, ws, lds, rb, 48 + (pr - 9) * 4, tf0);
    else               gemmA_body<3, 2, false>(wqkv, w1, b1, ws, lds, rb, 60 + (pr - 12) * 3, tf0);
}

constexpr int AT_K = 16384, AT_VKG = 672, AT_SLOT = AT_K + 16 * AT_VKG, AT_R = 3, AT_FLAG = AT_R * AT_SLOT;

template <bool SAFE>
DI void attn_loop(const char* ws, char* lds, int hd, int wid, int lane, const bf16x8 q0, const bf16x8 q1, f32x16& o, float& mref) {
    const unsigned lds0 = (unsigned)(uintptr_t)lds;
    const int b0 = (hd * 18) >> 4, h = lane >> 5, r = lane & 31;
    const char* kb0 = uni(ws + KF_OFF + (size_t)b0 * 65536 + wid * 1024);
    const char* vb0 = uni(ws + VT_OFF + (size_t)hd * 128 * 576 + wid * 2 * 576);
    const unsigned koff = lane * 16;
    const int vr = lane < 21 ? lane : lane - 21; const bool vact = lane < 39 && vr < 18;
    const unsigned voff = (lane < 21 ? 0 : 288) + vr * 16;
    auto issue = [&](int c) { const unsigned d = lds0 + (c % AT_R) * AT_SLOT;
        glds_s(koff, kb0 + c * 8192, __builtin_amdgcn_readfirstlane(d + wid * 1024));
        glds_s(koff, kb0 + 65536 + c * 8192, __builtin_amdgcn_readfirstlane(d + 8192 + wid * 1024));
        if (vact) { glds_s(voff, vb0 + c * 16 * 576, __builtin_amdgcn_readfirstlane(d + AT_K + (2 * wid) * AT_VKG));
                    glds_s(voff, vb0 + c * 16 * 576 + 576, __builtin_amdgcn_readfirstlane(d + AT_K + (2 * wid + 1) * AT_VKG)); } };
    const int reff = r < 19 ? r : ((r >= 20 && r <= 27) ? 20 : 19);
    const lds_cptr kp = (lds_cptr)lds + lane * 16;
    const lds_cptr vp = (lds_cptr)lds + AT_K + h * 336 + reff * 16;
    issue(0); issue(1);
    o = f32x16{};
    f32x16 negm = f32x16{};
    float m_run = -INFINITY;
    for (int c = 0; c < 8; ++c) {
        if (c == 7) wait_vm_bar<0>(); else wait_vm_bar<4>();
        if (c + 2 < 8) issue(c + 2);
        const int so = (c % AT_R) * AT_SLOT;
        if (!SAFE) {
            if (c == 0) {
                f32x16 s = MFMA32(ldsfrag(kp), q0, f32x16{}); s = MFMA32(ldsfrag(kp + 8192), q1, s);
                float m = s[0];
#pragma unroll
                for (int i = 1; i < 16; ++i) m = fmaxf(m, s[i]);
                auto sw = __builtin_amdgcn_permlane32_swap(__float_as_uint(m), __float_as_uint(m), false, false);
                m = fmaxf(__uint_as_float(sw[0]), __uint_as_float(sw[1]));
                mref = m;
#pragma unroll
                for (int i = 0; i < 16; ++i) negm[i] = -m;
            }
#pragma unroll
            for (int i = 0; i < 8; ++i) {
                f32x16 s = MFMA32(ldsfrag(kp + so + i * 1024), q0, negm); s = MFMA32(ldsfrag(kp + so + 8192 + i * 1024), q1, s);
                u32x4 p0, p1;
#pragma unroll
                for (int k = 0; k < 4; ++k) { p0[k] = cvtpk(__builtin_amdgcn_exp2f(s[2 * k]), __builtin_amdgcn_exp2f(s[2 * k + 1]));
                                              p1[k] = cvtpk(__builtin_amdgcn_exp2f(s[8 + 2 * k]), __builtin_amdgcn_exp2f(s[8 + 2 * k + 1])); }
                o = MFMA32(ldsfrag(vp + so + (2 * i) * AT_VKG), __builtin_bit_cast(bf16x8, p0), o);
                o = MFMA32(ldsfrag(vp + so + (2 * i + 1) * AT_VKG), __builtin_bit_cast(bf16x8, p1), o);
            }
        } else {
            for (int i = 0; i < 8; ++i) {
                f32x16 s = MFMA32(ldsfrag(kp + so + i * 1024), q0, f32x16{}); s = MFMA32(ldsfrag(kp + so + 8192 + i * 1024), q1, s);
                float m = s[0];
#pragma unroll
                for (int k = 1; k < 16; ++k) m = fmaxf(m, s[k]);
                auto sw = __builtin_amdgcn_permlane32_swap(__float_as_uint(m), __float_as_uint(m), false, false);
                m = fmaxf(fmaxf(__uint_as_float(sw[0]), __uint_as_float(sw[1])), m_run);
                const float alpha = __builtin_amdgcn_exp2f(m_run - m);
                m_run = m;
#pragma unroll
                for (int k = 0; k < 16; ++k) o[k] *= alpha;
                u32x4 p0, p1;
#pragma unroll
                for (int k = 0; k < 4; ++k) { p0[k] = cvtpk(__builtin_amdgcn_exp2f(s[2 * k] - m), __builtin_amdgcn_exp2f(s[2 * k + 1] - m));
                                              p1[k] = cvtpk(__builtin_amdgcn_exp2f(s[8 + 2 * k] - m), __builtin_amdgcn_exp2f(s[8 + 2 * k + 1] - m)); }
                o = MFMA32(ldsfrag(vp + so + (2 * i) * AT_VKG), __builtin_bit_cast(bf16x8, p0), o);
                o = MFMA32(ldsfrag(vp + so + (2 * i + 1) * AT_VKG), __builtin_bit_cast(bf16x8, p1), o);
            }
        }
    }
}

DI void attn_body(char* ws, char* lds, int hd, int qb) {
    const int tid = threadIdx.x, lane = tid & 63, wid = __builtin_amdgcn_readfirstlane(tid >> 6), h = lane >> 5;
    for (int i = tid; i < AT_R * 16 * 2 * 3; i += 512) { const int row = i % 3, hh = (i / 3) & 1, kg = (i / 6) & 15, sl = i / 96;
        const unsigned v = row == 0 ? 0x3f803f80u : 0u;
        *(u32x4*)(lds + sl * AT_SLOT + AT_K + kg * AT_VKG + hh * 336 + (18 + row) * 16) = (u32x4){v, v, v, v}; }
    if (tid == 0) *(volatile int*)(lds + AT_FLAG) = 0;
    const int b0 = (hd * 18) >> 4, off = (hd * 18) & 15, T = qb * 8 + wid;
    u32x4 qa = *(const u32x4*)(ws + QF_OFF + ((size_t)b0 * 64 + T) * 1024 + lane * 16);
    u32x4 qb_ = *(const u32x4*)(ws + QF_OFF + ((size_t)(b0 + 1) * 64 + T) * 1024 + lane * 16);
#pragma unroll
    for (int k = 0; k < 4; ++k) { const int c = 8 * h + 2 * k; if (c < off) qa[k] = 0u; if (c >= off + 2) qb_[k] = 0u; }
    const bf16x8 q0 = __builtin_bit_cast(bf16x8, qa), q1 = __builtin_bit_cast(bf16x8, qb_);
    __syncthreads();
    f32x16 o; float mref;
    attn_loop<false>(ws, lds, hd, wid, lane, q0, q1, o, mref);
    { float l = o[10]; auto sw = __builtin_amdgcn_permlane32_swap(__float_as_uint(l), __float_as_uint(l), false, false); l = __uint_as_float(sw[0]);
      float mx = fabsf(o[0]);
#pragma unroll
      for (int k = 1; k < 10; ++k) mx = fmaxf(mx, fabsf(o[k]));
      const bool bad = !(l < 1.0e30f) || !(mx < 1.0e37f) || !(l > 1.0e-30f);
      if (__any(bad) && lane == 0) *(volatile int*)(lds + AT_FLAG) = 1; }
    __syncthreads();
    if (*(volatile int*)(lds + AT_FLAG)) { __syncthreads(); attn_loop<true>(ws, lds, hd, wid, lane, q0, q1, o, mref); }
    float l = o[10]; { auto sw = __builtin_amdgcn_permlane32_swap(__float_as_uint(l), __float_as_uint(l), false, false); l = __uint_as_float(sw[0]); }
    const float inv = 1.0f / l;
    u32x4 ca, cb;
#pragma unroll
    for (int k = 0; k < 4; ++k) ca[k] = cvtpk(o[2 * k] * inv, o[2 * k + 1] * inv);
    cb[0] = h == 0 ? cvtpk(o[8] * inv, o[9] * inv) : 0u; cb[1] = 0u; cb[2] = 0u; cb[3] = 0u;
    char* ob = uni(ws + OF_OFF + ((size_t)hd * 128 + 2 * T) * 1024);
    const unsigned oo = ((lane >> 4) & 1) * 1024 + h * 256 + (lane & 15) * 16;
    gstore16(oo, ob, ca); gstore16(oo + 512, ob, cb);
}
__global__ __launch_bounds__(512, 2) void k_attn(char* ws) {
    extern __shared__ __attribute__((aligned(16))) char lds[];
    const int b = blockIdx.x, xcd = b & 7, j = b >> 3;
    attn_body(ws, lds, xcd * 4 + (j & 3), j >> 2);
}

constexpr int F2_ROW = 288, F2_STAGE = 64 * F2_ROW, F2_R = 4;
DI void ffn2_body(const u16* __restrict__ w2, char* ws, char* lds, int rb, int ng, int ks) {
    const int tid = threadIdx.x, lane = tid & 63, wid = __builtin_amdgcn_readfirstlane(tid >> 6);
    const unsigned lds0 = (unsigned)(uintptr_t)lds;
    if (wid >= 4) {
        const int hw = wid - 4; unsigned voff[5];
#pragma unroll
        for (int i = 0; i < 5; ++i) { const int ci = 288 * hw + 64 * i + lane, row = ci / 18, c = ci % 18; voff[i] = row * 1152 + c * 16; }
        const char* wb = uni((const char*)w2 + (size_t)ks * 1152 * 1152 + ng * 288);
        auto issue = [&](int t) { const unsigned d = lds0 + (t % F2_R) * F2_STAGE + hw * 288 * 16;
#pragma unroll
            for (int i = 0; i < 4; ++i) glds_s(voff[i], wb + (size_t)t * 64 * 1152, __builtin_amdgcn_readfirstlane(d + i * 1024));
            if (lane < 32) glds_s(voff[4], wb + (size_t)t * 64 * 1152, __builtin_amdgcn_readfirstlane(d + 4096)); };
        issue(0); issue(1); issue(2);
        for (int t = 0; t < 18; ++t) {
            if (t <= 15) wait_vm_bar<10>(); else if (t == 16) wait_vm_bar<5>(); else wait_vm_bar<0>();
            if (t + 3 < 18) issue(t + 3);
        }
    } else {
        constexpr int LAG = 3, PD = 4, NALL = 36 * 9;
        const int u0 = rb * 8 + 2 * wid;
        const char* hb = uni(ws + HF_OFF + ((size_t)u0 * 144 + ks * 36) * 1024);
        const unsigned hoff = lane * 16;
        bf16x8 hf[PD][2], wf[LAG + 1];
        f32x4 acc[9][2];
        auto h_issue = [&](auto SS) { constexpr int s = SS.value; gload16(hf[s % PD][0], hoff, hb + s * 1024); gload16(hf[s % PD][1], hoff, hb + (144 + s) * 1024); };
        static_for<0, PD - 1>([&](auto SS) { h_issue(SS); });
        const lds_cptr wp = (lds_cptr)lds + ((lane >> 4) * 4 + ((lane & 15) >> 2)) * F2_ROW + (lane & 3) * 8;
        auto frag_ptr = [&](auto I) { constexpr int i = I.value, s = i / 9, n = i % 9; return wp + ((s >> 1) % F2_R) * F2_STAGE + (s & 1) * 32 * F2_ROW + n * 32; };
        const f32x4 zero4 = {0.f, 0.f, 0.f, 0.f};
        wait_vm_bar<63>();
        static_for<0, LAG>([&](auto I) { wf[I.value] = trfrag(frag_ptr(I), 16 * F2_ROW); });
        static_for<0, NALL>([&](auto I) {
            constexpr int i = I.value, j = i + LAG, s = i / 9, n = i % 9;
            if constexpr (j < NALL) { if constexpr (j % 18 == 0) { SBAR(); wait_vm_bar<63>(); SBAR(); } wf[j % (LAG + 1)] = trfrag(frag_ptr(IC<j>{}), 16 * F2_ROW); }
            if constexpr (n == 0) {
                wait_vm<2 * ((s + PD - 2 < 36 ? s + PD - 2 : 35) - s)>();
                asm volatile("" : "+v"(hf[s % PD][0]), "+v"(hf[s % PD][1]));
                if constexpr (s + PD - 1 < 36) h_issue(IC<s + PD - 1>{});
                SBAR();
            }
            const bf16x8 w = wf[i % (LAG + 1)];
            if constexpr (s == 0) { acc[n][0] = MFMA16(w, hf[s % PD][0], zero4); acc[n][1] = MFMA16(w, hf[s % PD][1], zero4); }
            else { acc[n][0] = MFMA16(w, hf[s % PD][0], acc[n][0]); acc[n][1] = MFMA16(w, hf[s % PD][1], acc[n][1]); }
            SBAR();
        });
        char* gb = uni(ws + GP_OFF + ((((size_t)rb * 4 + ng) * 4 + wid) * 4 + ks) * 18 * 1024);
        static_for<0, 9>([&](auto N) { constexpr int n = N.value; gstore16(hoff, gb + (2 * n) * 1024, __builtin_bit_cast(u32x4, acc[n][0])); gstore16(hoff, gb + (2 * n + 1) * 1024, __builtin_bit_cast(u32x4, acc[n][1])); });
    }
}
__global__ __launch_bounds__(512, 2) void k_ffn2(const u16* __restrict__ w2, char* ws) {
    extern __shared__ __attribute__((aligned(16))) char lds[];
    const int b = blockIdx.x, xcd = b & 7, j = b >> 3;
    ffn2_body(w2, ws, lds, (xcd >> 2) * 8 + (j & 7), j >> 3, xcd & 3);
}

constexpr int WO_O = 0, WO_RING = 65536, WO_STAGE = 72 * 288, WO_R = 3;
DI void final_body(const u16* __restrict__ x, const u16* __restrict__ wo, const u16* __restrict__ b2, char* ws, u16* __restrict__ out, char* lds, int rb, int ng, int q) {
    const int tid = threadIdx.x, lane = tid & 63, wid = __builtin_amdgcn_readfirstlane(tid >> 6);
    const unsigned lds0 = (unsigned)(uintptr_t)lds;
    const int u0 = rb * 8 + 2 * q;
    unsigned voff[3];
#pragma unroll
    for (int i = 0; i < 3; ++i) { const int ci = 162 * wid + 64 * i + lane, row = ci / 18, c = ci % 18; voff[i] = row * 1152 + c * 16; }
    const char* wb = uni((const char*)wo + ng * 288);
    auto issue = [&](int t) { const unsigned d = lds0 + WO_RING + (t % WO_R) * WO_STAGE + wid * 162 * 16;
        glds_s(voff[0], wb + (size_t)t * 72 * 1152, __builtin_amdgcn_readfirstlane(d));
        glds_s(voff[1], wb + (size_t)t * 72 * 1152, __builtin_amdgcn_readfirstlane(d + 1024));
        if (lane < 34) glds_s(voff[2], wb + (size_t)t * 72 * 1152, __builtin_amdgcn_readfirstlane(d + 2048)); };
    { const char* ob = uni(ws + OF_OFF + ((size_t)(4 * wid) * 128 + u0) * 1024);
#pragma unroll
      for (int i = 0; i < 8; ++i) glds_s(lane * 16, ob + (size_t)(i >> 1) * 128 * 1024 + (i & 1) * 1024, __builtin_amdgcn_readfirstlane(lds0 + WO_O + (4 * wid * 2 + i) * 1024)); }
    issue(0); issue(1);
    const int g = lane >> 4, qq = (lane & 15) >> 2, p = lane & 3;
    const int r1 = g == 0 ? qq : g == 1 ? 4 + qq : 16 + (qq & 1), r2 = g == 0 ? 8 + qq : g == 1 ? 12 + qq : 16 + (qq & 1);
    const lds_cptr w1p = (lds_cptr)lds + WO_RING + r1 * 288 + p * 8, w2p = (lds_cptr)lds + WO_RING + r2 * 288 + p * 8;
    const lds_cptr op = (lds_cptr)lds + WO_O + lane * 16;
    f32x4 acc[2] = {{0.f, 0.f, 0.f, 0.f}, {0.f, 0.f, 0.f, 0.f}}, accx = {0.f, 0.f, 0.f, 0.f};
    const int xu = wid & 1;
    for (int t = 0; t < 8; ++t) {
        if (t == 7) wait_vm_bar<0>(); else wait_vm_bar<3>();
        if (t + 2 < 8) issue(t + 2);
        const int so = (t % WO_R) * WO_STAGE;
#pragma unroll
        for (int k = 0; k < 4; ++k) { const int hd = 4 * t + k;
            const bf16x8 o0 = ldsfrag(op + hd * 2048), o1 = ldsfrag(op + hd * 2048 + 1024);
            s16x4 lo = vtr(w1p + so + k * 18 * 288 + wid * 32), hi = vtr(w2p + so + k * 18 * 288 + wid * 32);
            const bf16x8 w = __builtin_shufflevector(lo, hi, 0, 1, 2, 3, 4, 5, 6, 7);
            acc[0] = MFMA16(w, o0, acc[0]); acc[1] = MFMA16(w, o1, acc[1]);
            if (wid < 2) { s16x4 lo2 = vtr(w1p + so + k * 18 * 288 + 8 * 32), hi2 = vtr(w2p + so + k * 18 * 288 + 8 * 32);
                const bf16x8 w8 = __builtin_shufflevector(lo2, hi2, 0, 1, 2, 3, 4, 5, 6, 7);
                accx = MFMA16(w8, xu ? o1 : o0, accx); }
        }
    }
    auto fin = [&](f32x4 s, int nt, int u) {
        const char* gb = ws + GP_OFF + (((size_t)rb * 4 + ng) * 4 + q) * 4 * 18 * 1024 + (2 * nt + u) * 1024 + lane * 16;
        f32x4 gsum = *(const f32x4*)gb;
#pragma unroll
        for (int k = 1; k < 4; ++k) gsum += *(const f32x4*)(gb + (size_t)k * 18 * 1024);
        const int col = ng * 144 + nt * 16 + g * 4, tok = (u0 + u) * 16 + (lane & 15);
        const u32x2 bb = *(const u32x2*)(b2 + col), xx = *(const u32x2*)(x + (size_t)tok * DM + col);
        const float bv[4] = {bf_lo(bb[0]), bf_hi(bb[0]), bf_lo(bb[1]), bf_hi(bb[1])}, xv[4] = {bf_lo(xx[0]), bf_hi(xx[0]), bf_lo(xx[1]), bf_hi(xx[1])};
        float o[4];
#pragma unroll
        for (int i = 0; i < 4; ++i) { const float gg = gsum[i] + bv[i]; const float sg = gg / (1.0f + __expf(-gg)); o[i] = xv[i] + s[i] * sg; }
        *(u32x2*)(out + (size_t)tok * DM + col) = (u32x2){cvtpk(o[0], o[1]), cvtpk(o[2], o[3])}; };
    fin(acc[0], wid, 0); fin(acc[1], wid, 1);
    if (wid < 2) fin(accx, 8, xu);
}
__global__ __launch_bounds__(512, 2) void k_final(const u16* __restrict__ x, const u16* __restrict__ wo, const u16* __restrict__ b2, char* ws, u16* __restrict__ out) {
    extern __shared__ __attribute__((aligned(16))) char lds[];
    const int b = blockIdx.x, xcd = b & 7, j = b >> 3;
    final_body(x, wo, b2, ws, out, lds, (xcd >> 2) * 8 + (j & 7), j >> 3, xcd & 3);
}

extern "C" void kernel_launch(void* const* d_in, const int* in_sizes, int n_in, void* d_out, int out_size, void* d_ws, size_t ws_size, hipStream_t stream) {
    const u16* x = (const u16*)d_in[0]; const u16* wqkv = (const u16*)d_in[1]; const u16* wo = (const u16*)d_in[2];
    const u16* w1 = (const u16*)d_in[4]; const u16* b1 = (const u16*)d_in[5]; const u16* w2 = (const u16*)d_in[6]; const u16* b2 = (const u16*)d_in[7];
    char* ws = (char*)d_ws; u16* out = (u16*)d_out;
    constexpr int L_A = GA_BIAS + 160 * 4, L_B = AT_FLAG + 16, L_C = F2_R * F2_STAGE, L_D = WO_RING + WO_R * WO_STAGE;
    (void)hipFuncSetAttribute((const void*)k_gemmA, hipFuncAttributeMaxDynamicSharedMemorySize, L_A);
    (void)hipFuncSetAttribute((const void*)k_attn, hipFuncAttributeMaxDynamicSharedMemorySize, L_B);
    (void)hipFuncSetAttribute((const void*)k_ffn2, hipFuncAttributeMaxDynamicSharedMemorySize, L_C);
    (void)hipFuncSetAttribute((const void*)k_final, hipFuncAttributeMaxDynamicSharedMemorySize, L_D);
    k_ln<<<256, 64, 0, stream>>>(x, ws);
    k_gemmA<<<256, 512, L_A, stream>>>(wqkv, w1, b1, ws);
    k_attn<<<256, 512, L_B, stream>>>(ws);
    k_ffn2<<<256, 512, L_C, stream>>>(w2, ws);
    k_final<<<256, 512, L_D, stream>>>(x, wo, b2, ws, out);
}
```

```cpp
#include <hip/hip_runtime.h>
#include <stdint.h>
#include <utility>

#define DI __device__ __forceinline__
#define LAS __attribute__((address_space(3)))
#define GAS __attribute__((address_space(1)))
typedef short bf16x8 __attribute__((ext_vector_type(8)));
typedef short s16x4 __attribute__((ext_vector_type(4)));
typedef float f32x2 __attribute__((ext_vector_type(2)));
typedef float f32x4 __attribute__((ext_vector_type(4)));
typedef float f32x16 __attribute__((ext_vector_type(16)));
typedef unsigned u32x2 __attribute__((ext_vector_type(2)));
typedef unsigned u32x4 __attribute__((ext_vector_type(4)));
typedef __bf16 bf16x2_t __attribute__((ext_vector_type(2)));
typedef unsigned short u16;
typedef unsigned long long u64;

constexpr int DM = 576;
constexpr size_t XN_OFF = 0;
constexpr size_t QF_OFF = XN_OFF + 2359296;
constexpr size_t KF_OFF = QF_OFF + 2359296;
constexpr size_t VT_OFF = KF_OFF + 2359296;
constexpr size_t HF_OFF = VT_OFF + 2359296;
constexpr size_t OF_OFF = HF_OFF + 18874368;
constexpr size_t OT_OFF = OF_OFF + 2097152;
constexpr size_t GP_OFF = OT_OFF + 262144;
constexpr size_t FL_OFF = GP_OFF + 18874368;
constexpr size_t FL_LN = FL_OFF + 4096, FL_QKV = FL_OFF + 8192, FL_H = FL_OFF + 12288, FL_O = FL_OFF + 16384, FL_G = FL_OFF + 20480;
constexpr size_t PROF_OFF = 128u << 20;
#define STAMP(I) do {} while (0)
DI unsigned cvtpk(float lo, float hi) { f32x2 v = {lo, hi}; bf16x2_t b = __builtin_convertvector(v, bf16x2_t); return __builtin_bit_cast(unsigned, b); }
DI float bf_lo(unsigned w) { return __uint_as_float(w << 16); }
DI float bf_hi(unsigned w) { return __uint_as_float(w & 0xffff0000u); }
DI float rbf(float x) { return bf_lo(cvtpk(x, x)); }
template <int V> struct IC { static constexpr int value = V; };
template <int B, typename Fn, int... Is> DI void static_for_impl(Fn& fn, std::integer_sequence<int, Is...>) { (fn(IC<B + Is>{}), ...); }
template <int B, int E, typename Fn> DI void static_for(Fn&& fn) { if constexpr (B < E) static_for_impl<B>(fn, std::make_integer_sequence<int, E - B>{}); }
#define SBAR() __builtin_amdgcn_sched_barrier(0)
template <typename T> DI void pin_(T& x) { asm volatile("" : "+v"(x)); }
#define PIN(x) pin_(x)
#define MFMA16(a, b, c) __builtin_amdgcn_mfma_f32_16x16x32_bf16((a), (b), (c), 0, 0, 0)
DI void mfma16_acc(f32x4& acc, const bf16x8& a, const bf16x8& b) { asm volatile("v_mfma_f32_16x16x32_bf16 %0, %1, %2, %0" : "+v"(acc) : "v"(a), "v"(b)); }
DI void mfma16_zero(f32x4& acc, const bf16x8& a, const bf16x8& b) { asm volatile("v_mfma_f32_16x16x32_bf16 %0, %1, %2, 0" : "=&v"(acc) : "v"(a), "v"(b)); }
DI void mfma16_init(f32x4& acc, const bf16x8& a, const bf16x8& b, const f32x4& c) { asm volatile("s_nop 1\n\tv_mfma_f32_16x16x32_bf16 %0, %1, %2, %3" : "=&v"(acc) : "v"(a), "v"(b), "v"(c)); }
#define MFMA_DRAIN() asm volatile("s_nop 7\n\ts_nop 7" ::: "memory")
#define MFMA32(a, b, c) __builtin_amdgcn_mfma_f32_32x32x16_bf16((a), (b), (c), 0, 0, 0)

DI void glds_in(unsigned voff, const void* sbase, unsigned lds_base) {
    asm volatile("s_mov_b32 m0, %2\n\ts_nop 0\n\tglobal_load_lds_dwordx4 %0, %1" :: "v"(voff), "s"(sbase), "s"(lds_base) : "memory"); }
DI void glds_in_m(unsigned voff, const void* sbase, unsigned lds_base, u64 mask) {
    asm volatile("s_mov_b32 m0, %2\n\ts_mov_b64 exec, %3\n\tglobal_load_lds_dwordx4 %0, %1\n\ts_mov_b64 exec, -1" :: "v"(voff), "s"(sbase), "s"(lds_base), "s"(mask) : "memory"); }
DI void glds_in_vm(const void* g, unsigned lds_base, u64 mask) {
    asm volatile("s_mov_b32 m0, %1\n\ts_mov_b64 exec, %2\n\tglobal_load_lds_dwordx4 %0, off\n\ts_mov_b64 exec, -1" :: "v"(g), "s"(lds_base), "s"(mask) : "memory"); }
DI void glds_ho(unsigned voff, const void* sbase, unsigned lds_base) {
    asm volatile("s_mov_b32 m0, %2\n\ts_nop 0\n\tglobal_load_lds_dwordx4 %0, %1 sc1" :: "v"(voff), "s"(sbase), "s"(lds_base) : "memory"); }
DI void glds_ho_m(unsigned voff, const void* sbase, unsigned lds_base, u64 mask) {
    asm volatile("s_mov_b32 m0, %2\n\ts_mov_b64 exec, %3\n\tglobal_load_lds_dwordx4 %0, %1 sc1\n\ts_mov_b64 exec, -1" :: "v"(voff), "s"(sbase), "s"(lds_base), "s"(mask) : "memory"); }
template <typename T> DI void gload16_ho(T& d, unsigned voff, const void* sbase) { asm volatile("global_load_dwordx4 %0, %1, %2 sc1" : "=v"(d) : "v"(voff), "s"(sbase) : "memory"); }
DI void gload8_in(u32x2& d, unsigned voff, const void* sbase) { asm volatile("global_load_dwordx2 %0, %1, %2" : "=v"(d) : "v"(voff), "s"(sbase) : "memory"); }
DI void gstore16_ho(unsigned voff, void* sbase, u32x4 v) { asm volatile("global_store_dwordx4 %0, %1, %2 sc1\n\ts_nop 1" :: "v"(voff), "v"(v), "s"(sbase) : "memory"); }
template <int N> DI void wait_vm() { asm volatile("s_waitcnt vmcnt(%0)" :: "n"(N) : "memory"); }
template <int N> DI void wait_vm_bar() { asm volatile("s_waitcnt vmcnt(%0) lgkmcnt(0)\n\ts_barrier" :: "n"(N) : "memory"); }
typedef short v4i16_t __attribute__((ext_vector_type(4)));
typedef LAS const char* lds_cptr;
DI s16x4 vtr(lds_cptr p) { return __builtin_bit_cast(s16x4, __builtin_amdgcn_ds_read_tr16_b64_v4i16((LAS v4i16_t*)p)); }
DI bf16x8 trfrag(lds_cptr p, int off2) { s16x4 lo = vtr(p), hi = vtr(p + off2); return __builtin_shufflevector(lo, hi, 0, 1, 2, 3, 4, 5, 6, 7); }
DI bf16x8 ldsfrag(lds_cptr p) { return *(const LAS bf16x8*)p; }
template <typename T> DI T* uni(T* p) {
    uint64_t a = (uint64_t)p; unsigned lo = __builtin_amdgcn_readfirstlane((unsigned)a), hi = __builtin_amdgcn_readfirstlane((unsigned)(a >> 32));
    return (T*)(((uint64_t)hi << 32) | lo); }

DI void flag_set(char* ws, size_t arr, int idx, u64 tag) { __hip_atomic_store((GAS u64*)(ws + arr) + idx, tag, __ATOMIC_RELAXED, __HIP_MEMORY_SCOPE_AGENT); }
DI void flag_wait(char* ws, size_t arr, int idx, bool act, u64 tag) {
    GAS u64* p = (GAS u64*)(ws + arr) + (act ? idx : 0);
    for (unsigned spin = 0; spin < (1u << 22); ++spin) {
        const u64 v = __hip_atomic_load(p, __ATOMIC_RELAXED, __HIP_MEMORY_SCOPE_AGENT);
        if (__all(!act || v == tag)) break;
        __builtin_amdgcn_s_sleep(1);
    }
}
DI void touch(const void* g, unsigned lds_dummy, u64 mask) {
    asm volatile("s_mov_b32 m0, %1\n\ts_mov_b64 exec, %2\n\tglobal_load_lds_dword %0, off\n\ts_mov_b64 exec, -1" :: "v"(g), "s"(lds_dummy), "s"(mask) : "memory"); }
DI void flag_to_lds(unsigned voff, const void* sbase, unsigned lds_dst, u64 mask) {
    asm volatile("s_mov_b32 m0, %2\n\ts_mov_b64 exec, %3\n\tglobal_load_lds_dword %0, %1 sc1\n\ts_mov_b64 exec, -1" :: "v"(voff), "s"(sbase), "s"(lds_dst), "s"(mask) : "memory"); }
DI void flag_load(u32x2& d, const char* ws, size_t arr, int idx) { asm volatile("global_load_dwordx2 %0, %1, %2 sc1" : "=v"(d) : "v"(idx * 8), "s"(ws + arr) : "memory"); }
DI bool flag_all(const u32x2& v, bool act, u64 tag) { return __all(!act || (v[0] == (unsigned)tag && v[1] == (unsigned)(tag >> 32))); }
#define BAR0() asm volatile("s_barrier" ::: "memory")
#define BAR() asm volatile("s_waitcnt lgkmcnt(0)\n\ts_barrier" ::: "memory")

struct Params { const u16* x; const u16* wqkv; const u16* wo; const u16* w1; const u16* b1; const u16* w2; const u16* b2; char* ws; u16* out; };

constexpr int GA_ROW = 224, GA_SLOT = 192 * GA_ROW, GA_BIAS = 3 * GA_SLOT;
constexpr int LN_PART = GA_BIAS + 1024, LN_STAGE = LN_PART + 1024;
constexpr int AT_K = 16384, AT_VKG = 672, AT_SLOT = AT_K + 16 * AT_VKG, AT_R = 3;
constexpr int CD_O = 0, CD_OWN = 36864, CD_RING = 67584, CD_ROW = 288, CD_STAGE = 64 * CD_ROW;
constexpr int MISC = 160000, LDS_BYTES = 163840;
constexpr float QSCALE = 0.23570226039551584f * 1.4426950408889634f;

template <int CTRL> DI float dpp_add(float v) { return v + __int_as_float(__builtin_amdgcn_update_dpp(0, __float_as_int(v), CTRL, 0xf, 0xf, true)); }
DI float sum8(float v) { v = dpp_add<0xB1>(v); v = dpp_add<0x4E>(v); return dpp_add<0x141>(v); }
DI void ln_compute(char* lds, int wid, int lane, const u32x2 (&a)[2], const u32x2 (&b)[2]) {
    const int r = lane >> 3, c = lane & 7; const bool two = wid == 1;
    float v[2][8];
#pragma unroll
    for (int i = 0; i < 2; ++i) { v[i][0] = bf_lo(a[i][0]); v[i][1] = bf_hi(a[i][0]); v[i][2] = bf_lo(a[i][1]); v[i][3] = bf_hi(a[i][1]);
                                  v[i][4] = bf_lo(b[i][0]); v[i][5] = bf_hi(b[i][0]); v[i][6] = bf_lo(b[i][1]); v[i][7] = bf_hi(b[i][1]); }
    float* part = (float*)(lds + LN_PART);
    float s = ((v[0][0] + v[0][1]) + (v[0][2] + v[0][3])) + ((v[0][4] + v[0][5]) + (v[0][6] + v[0][7]));
    if (two) s += ((v[1][0] + v[1][1]) + (v[1][2] + v[1][3])) + ((v[1][4] + v[1][5]) + (v[1][6] + v[1][7]));
    s = sum8(s);
    if (c == 0) part[r * 8 + wid] = s;
    BAR();
    const f32x4 p0 = *(const f32x4*)(part + r * 8), p1 = *(const f32x4*)(part + r * 8 + 4);
    const float mean = (((p0[0] + p0[1]) + (p0[2] + p0[3])) + ((p1[0] + p1[1]) + (p1[2] + p1[3]))) * (1.0f / DM);
    float q = 0.f;
#pragma unroll
    for (int k = 0; k < 8; ++k) { const float d = v[0][k] - mean; q += d * d; }
    if (two) {
#pragma unroll
        for (int k = 0; k < 8; ++k) { const float d = v[1][k] - mean; q += d * d; } }
    q = sum8(q);
    if (c == 0) part[64 + r * 8 + wid] = q;
    BAR();
    const f32x4 q0 = *(const f32x4*)(part + 64 + r * 8), q1 = *(const f32x4*)(part + 64 + r * 8 + 4);
    const float var = (((q0[0] + q0[1]) + (q0[2] + q0[3])) + ((q1[0] + q1[1]) + (q1[2] + q1[3]))) * (1.0f / DM);
    const float m_bf = rbf(mean), den = rbf(sqrtf(rbf(rbf(var) + 1e-5f))), inv = 1.0f / den;
#pragma unroll
    for (int i = 0; i < 2; ++i) if (i == 0 || two) { u32x4 o;
#pragma unroll
        for (int k = 0; k < 4; ++k) o[k] = cvtpk(rbf(v[i][2 * k] - m_bf) * inv, rbf(v[i][2 * k + 1] - m_bf) * inv);
        *(u32x4*)(lds + LN_STAGE + (i ? 8 : wid) * 1024 + lane * 16) = o; }
    BAR();
}

constexpr int GA_LAG = 3, GA_XD = 4;
template <int NT1> struct GaSched {
    static constexpr int NT2 = 6, LAG = GA_LAG, XD = GA_XD, N1 = 18 * NT1, NALL = N1 + 18 * NT2, NST = 6;
    static constexpr int ks_of(int i) { return i < N1 ? i / NT1 : 18 + (i - N1) / NT2; }
    static constexpr int n_of(int i) { return i < N1 ? i % NT1 : (i - N1) % NT2; }
    static constexpr int first_frag(int ks) { return ks < 18 ? ks * NT1 : N1 + (ks - 18) * NT2; }
    static constexpr int sync_step(int st) { return st == 0 ? -1 : st >= NST ? NALL : first_frag(6 * st) - LAG; }
    int sync[NALL] = {}, syncN[NALL] = {}, xw[NALL] = {}, xwN[NALL] = {}, gst[NALL] = {}, gpair[NALL] = {}, xis[NALL] = {}, prio[NALL] = {};
    constexpr GaSched() {
        int posG[NST + 1] = {}, posX[18] = {}, issued = 0;
        for (int i = 0; i < NALL; ++i) { sync[i] = -1; xw[i] = -1; gst[i] = -1; xis[i] = -1; prio[i] = -1; }
        for (int xs = 0; xs < XD; ++xs) { issued += 2; posX[xs] = issued; }
        for (int st = 2; st + 1 < NST; ++st) { const int a = sync_step(st) + 1, len = sync_step(st + 1) - a;
            for (int p = 0; p < 3; ++p) { const int at = a + 4 + (p * (len - 10)) / 3; gst[at] = st + 1; gpair[at] = p; } }
        for (int ks = 0; ks + XD < 18; ++ks) xis[first_frag(ks) + 2] = ks + XD;
        int last = -1;
        for (int i = 0; i < NALL; ++i) {
            for (int st = 1; st < NST; ++st) if (sync_step(st) == i) { sync[i] = st; syncN[i] = st < 3 ? 63 : issued - posG[st]; }
            if (i < N1 && n_of(i) == 0) { xw[i] = ks_of(i); xwN[i] = issued - posX[ks_of(i)]; }
            if (gst[i] >= 0) { issued += 2; posG[gst[i]] = issued; }
            if (xis[i] >= 0) { issued += 2; posX[xis[i]] = issued; }
            if (i == N1 - 1) issued += NT1;
            int st = 0; while (st + 1 < NST && sync_step(st + 1) <= i) ++st;
            const int a = sync_step(st), b = sync_step(st + 1), num = (i - a) * 100 / (b - a);
            const int pr = num < 45 ? 3 : num < 75 ? 2 : num < 92 ? 1 : 0;
            if (pr != last) { prio[i] = pr; last = pr; }
        }
    }
    constexpr bool stores_covered() const { int at = -1; for (int i = 0; i < NALL; ++i) if (gst[i] == 4 && gpair[i] == 2) at = i; return at > N1 - 1; }
};
struct GaRole { int nq, nf1, tq0, tf0; bool isv; };
DI GaRole ga_role(int cg) {
    const int pr = cg >> 1; GaRole r; r.tf0 = pr * 18 + (cg & 1) * 10; r.isv = false;
    if ((cg & 1) == 0) { r.nq = 3; r.nf1 = 4; r.tq0 = pr * 3; }
    else if (pr < 9)   { r.nq = 4; r.nf1 = 2; r.tq0 = 72 + pr * 4; r.isv = true; }
    else if (pr < 12)  { r.nq = 4; r.nf1 = 2; r.tq0 = 48 + (pr - 9) * 4; }
    else               { r.nq = 3; r.nf1 = 2; r.tq0 = 60 + (pr - 12) * 3; }
    return r; }
DI void ga_prefetch(const Params& P, unsigned lds0, int wid, int lane, const GaRole& R) {
    if (wid == 0) return;
    if (wid == 2) glds_in_m(lane * 16, (const char*)P.b1 + R.tf0 * 32, lds0 + GA_BIAS, (1ull << (2 * (R.nf1 + 6))) - 1);
    const char* src[2]; unsigned inc[2]; u64 m[2];
#pragma unroll
    for (int i = 0; i < 2; ++i) { const int ci = 128 * (wid - 1) + 64 * i + lane, row = (ci * 4682) >> 16, c = ci - row * 14, unit = c >> 1, half = c & 1; const bool isq = unit < R.nq;
        const unsigned off = isq ? (unsigned)row * 3456u + (R.tq0 + unit) * 32 + half * 16 : (unsigned)row * 9216u + (R.tf0 + unit - R.nq) * 32 + half * 16;
        src[i] = (isq ? (const char*)P.wqkv : (const char*)P.w1) + off; inc[i] = isq ? 64 * 3456 : 64 * 9216; m[i] = __ballot(unit < R.nq + R.nf1); }
    const unsigned d0 = __builtin_amdgcn_readfirstlane(lds0 + (wid - 1) * 2048);
#pragma unroll
    for (int p = 0; p < 9; ++p) { glds_in_vm(src[0], d0 + p * 64 * GA_ROW, m[0]); glds_in_vm(src[1], d0 + p * 64 * GA_ROW + 1024, m[1]); src[0] += inc[0]; src[1] += inc[1]; }
}

template <int NQ, int NF1, bool ISV, typename Hook>
DI void gemmA_body(const Params& P, char* lds, int rb, int tq0, int tf0, int myflag, u64 tag, Hook& hook) {
    constexpr int NT1 = NQ + NF1, NT2 = 6;
    using SC = GaSched<NT1>; constexpr SC sc{}; constexpr int LAG = SC::LAG, N1 = SC::N1, NALL = SC::NALL, XD = SC::XD;
    static_assert(sc.stores_covered(), "publish point");
    char* ws = P.ws;
    const int tid = threadIdx.x, lane = tid & 63, wid = __builtin_amdgcn_readfirstlane(tid >> 6);
    const unsigned lds0 = (unsigned)(uintptr_t)lds;
    const int T = rb * 8 + wid;
    bf16x8 xf[18][2];
    const char* xbase = uni(ws + XN_OFF + (size_t)(2 * T) * 18 * 1024);
    const unsigned xoff = lane * 16;
    auto x_issue = [&](auto SS) { constexpr int s = SS.value; gload16_ho(xf[s][0], xoff, xbase + s * 1024); gload16_ho(xf[s][1], xoff, xbase + (18 + s) * 1024); };
    static_for<0, XD>([&](auto S) { x_issue(S); });
    unsigned off2[2]; u64 m2[2];
    const unsigned sdst = __builtin_amdgcn_readfirstlane(lds0 + wid * 112 * 16);
    auto g_issue = [&](auto ST, auto PP) { constexpr int st = ST.value, p = PP.value;
        if constexpr (st == 3 && p == 0) {
            int l2 = threadIdx.x & 63; asm volatile("" : "+v"(l2));
#pragma unroll
            for (int i = 0; i < 2; ++i) { const int ci = 112 * wid + 64 * i + l2, row = (ci * 4682) >> 16, c = ci - row * 14;
                off2[i] = (unsigned)row * 9216u + c * 16; m2[i] = __ballot((i == 0 || l2 < 48) && c < 2 * NT2); } }
        const unsigned d = sdst + (st % 3) * GA_SLOT + p * 64 * GA_ROW;
        const char* sb = uni((const char*)P.w1 + (size_t)((st - 3) * 3 + p) * 64 * 9216 + (tf0 + NF1) * 32);
        glds_in_m(off2[0], sb, d, m2[0]); glds_in_m(off2[1], sb, d + 1024, m2[1]); };
    lds_cptr wp0 = (lds_cptr)lds + ((lane >> 4) * 4 + ((lane & 15) >> 2)) * GA_ROW + (lane & 3) * 8, wp1 = wp0 + GA_SLOT, wp2 = wp0 + 2 * GA_SLOT;
    PIN(wp0); PIN(wp1); PIN(wp2);
    const lds_cptr bp = (lds_cptr)lds + GA_BIAS + (lane >> 4) * 8;
    f32x4 acc[7][2];
    bf16x8 wf[LAG + 1];
    auto frag_ptr = [&](auto I) { constexpr int i = I.value, ks = SC::ks_of(i), n = SC::n_of(i), st = ks / 6;
        return (st % 3 == 0 ? wp0 : st % 3 == 1 ? wp1 : wp2) + (ks - 6 * st) * 32 * GA_ROW + n * 32; };
    auto epi_h = [&](int n, int tf) {
#pragma unroll
        for (int u = 0; u < 2; ++u) { u32x4 o;
#pragma unroll
            for (int k = 0; k < 2; ++k) { float a0 = fmaxf(acc[n][u][2 * k], 0.f), a1 = fmaxf(acc[n][u][2 * k + 1], 0.f), b0 = fmaxf(acc[n + 1][u][2 * k], 0.f), b1_ = fmaxf(acc[n + 1][u][2 * k + 1], 0.f);
                o[k] = cvtpk(a0 * a0, a1 * a1); o[2 + k] = cvtpk(b0 * b0, b1_ * b1_); }
            gstore16_ho(xoff, uni(ws + HF_OFF + ((size_t)(2 * T + u) * 144 + (tf >> 1)) * 1024), o); } };
    auto epi_qk = [&](int n, int tq) {
        const float sc_ = tq < 36 ? QSCALE : 1.0f;
        unsigned p0x = cvtpk(acc[n][0][0] * sc_, acc[n][0][1] * sc_), p0y = cvtpk(acc[n][0][2] * sc_, acc[n][0][3] * sc_);
        unsigned p1x = cvtpk(acc[n][1][0] * sc_, acc[n][1][1] * sc_), p1y = cvtpk(acc[n][1][2] * sc_, acc[n][1][3] * sc_);
        auto sx = __builtin_amdgcn_permlane16_swap(p0x, p1x, false, false); auto sy = __builtin_amdgcn_permlane16_swap(p0y, p1y, false, false);
        u32x4 o = {sx[0], sy[0], sx[1], sy[1]};
        gstore16_ho(xoff, uni(ws + QF_OFF + ((size_t)tq * 64 + T) * 1024), o); };
    auto epi_v = [&](int n, int tq) {
        unsigned p0x = cvtpk(acc[n][0][0], acc[n][0][1]), p0y = cvtpk(acc[n][0][2], acc[n][0][3]);
        unsigned p1x = cvtpk(acc[n][1][0], acc[n][1][1]), p1y = cvtpk(acc[n][1][2], acc[n][1][3]);
        auto sx = __builtin_amdgcn_permlane32_swap(p0x, p1x, false, false); auto sy = __builtin_amdgcn_permlane32_swap(p0y, p1y, false, false);
        u32x4 o = {sx[0], sy[0], sx[1], sy[1]};
        const int col = (tq - 72) * 16 + (lane & 15), hd = col / 18, r = col - hd * 18, kg = 2 * T + (lane >> 5), h = (lane >> 4) & 1;
        gstore16_ho((unsigned)((((hd * 128 + kg) * 2 + h) * 18 + r) * 16), uni(ws + VT_OFF), o); };
    static_for<0, LAG>([&](auto I) { wf[I.value] = trfrag(frag_ptr(I), 16 * GA_ROW); });
    static_for<0, NALL>([&](auto I) {
        constexpr int i = I.value, j = i + LAG;
        if constexpr (sc.prio[i] >= 0) __builtin_amdgcn_s_setprio(sc.prio[i]);
        if constexpr (sc.sync[i] >= 0) { SBAR(); wait_vm_bar<sc.syncN[i]>();
            if constexpr (sc.sync[i] == 4) { if (tid == 0) flag_set(ws, FL_QKV, myflag, tag); }
            if constexpr (sc.sync[i] == 5) hook.ask();
            SBAR(); }
        if constexpr (i == SC::sync_step(5) + 12) { SBAR(); hook.check(); SBAR(); }
        if constexpr (i == SC::sync_step(5) + 14) { SBAR(); hook.go(); SBAR(); }
        if constexpr (i > SC::sync_step(5) + 14 && i <= SC::sync_step(5) + 34 && ((i - SC::sync_step(5)) & 1) == 0) { SBAR(); hook.template step<(i - SC::sync_step(5) - 16) / 2>(); SBAR(); }
        if constexpr (j < NALL) wf[j % (LAG + 1)] = trfrag(frag_ptr(IC<j>{}), 16 * GA_ROW);
        constexpr bool p1 = i < N1; constexpr int s = SC::ks_of(i) % 18, n = SC::n_of(i);
        if constexpr (sc.xw[i] >= 0) { wait_vm<sc.xwN[i]>(); PIN(xf[s][0]); PIN(xf[s][1]); }
        const bf16x8 w = wf[i % (LAG + 1)];
        constexpr bool isq = p1 && n < NQ;
        if constexpr (s == 0) {
            if constexpr (isq) { if constexpr (ISV) { mfma16_zero(acc[n][0], xf[s][0], w); mfma16_zero(acc[n][1], xf[s][1], w); } else { mfma16_zero(acc[n][0], w, xf[s][0]); mfma16_zero(acc[n][1], w, xf[s][1]); } }
            else { constexpr int bi = p1 ? n - NQ : NF1 + n; const u32x2 bb = *(const LAS u32x2*)(bp + bi * 32); const f32x4 c0 = {bf_lo(bb[0]), bf_hi(bb[0]), bf_lo(bb[1]), bf_hi(bb[1])};
                mfma16_init(acc[n][0], w, xf[s][0], c0); mfma16_init(acc[n][1], w, xf[s][1], c0); } }
        else if constexpr (isq && ISV) { mfma16_acc(acc[n][0], xf[s][0], w); mfma16_acc(acc[n][1], xf[s][1], w); }
        else { mfma16_acc(acc[n][0], w, xf[s][0]); mfma16_acc(acc[n][1], w, xf[s][1]); }
        SBAR();
        if constexpr (sc.gst[i] >= 0) { g_issue(IC<sc.gst[i]>{}, IC<sc.gpair[i]>{}); SBAR(); }
        if constexpr (sc.xis[i] >= 0) { x_issue(IC<sc.xis[i]>{}); SBAR(); }
        if constexpr (i == N1 - 1) {
            STAMP(5);
            MFMA_DRAIN();
            static_for<0, NT1>([&](auto Q) { PIN(acc[Q.value][0]); PIN(acc[Q.value][1]); });
            static_for<0, NQ>([&](auto Q) { if constexpr (ISV) epi_v(Q.value, tq0 + Q.value); else epi_qk(Q.value, tq0 + Q.value); });
            static_for<0, NF1 / 2>([&](auto Q) { epi_h(NQ + 2 * Q.value, tf0 + 2 * Q.value); });
            STAMP(6);
            SBAR();
        }
    });
    STAMP(7);
    __builtin_amdgcn_s_setprio(0);
    MFMA_DRAIN();
    static_for<0, NT2>([&](auto Q) { PIN(acc[Q.value][0]); PIN(acc[Q.value][1]); });
    static_for<0, NT2 / 2>([&](auto Q) { epi_h(2 * Q.value, tf0 + NF1 + 2 * Q.value); });
}

struct AttnIssue {
    const char* kb0; const char* vb0; unsigned koff, voff, lds0; u64 vmask;
    template <int PART> DI void part(int c, int wid) const { const unsigned d = lds0 + (c % AT_R) * AT_SLOT;
        if constexpr (PART < 2) glds_ho(koff, kb0 + PART * 65536 + c * 8192, d + PART * 8192);
        else glds_ho_m(voff, vb0 + c * 16 * 576 + (PART - 2) * 576, d - wid * 1024 + AT_K + (2 * wid + PART - 2) * AT_VKG, vmask); }
    DI void all(int c, int wid) const { part<0>(c, wid); part<1>(c, wid); part<2>(c, wid); part<3>(c, wid); }
};
DI float half_max(float m) { auto sw = __builtin_amdgcn_permlane32_swap(__float_as_uint(m), __float_as_uint(m), false, false); return fmaxf(__uint_as_float(sw[0]), __uint_as_float(sw[1])); }

template <typename Hook>
DI void attn_fast(const AttnIssue& issue, int wid, lds_cptr kp, lds_cptr vp, const bf16x8 q0, const bf16x8 q1, f32x16& o, bool pre, Hook& hook) {
    if (pre) wait_vm_bar<10>(); else wait_vm_bar<4>();
    f32x16 negm;
    { f32x16 s = MFMA32(ldsfrag(kp), q0, f32x16{}); s = MFMA32(ldsfrag(kp + 8192), q1, s);
      float m = s[0];
#pragma unroll
      for (int i = 1; i < 16; ++i) m = fmaxf(m, s[i]);
      m = half_max(m);
#pragma unroll
      for (int i = 0; i < 16; ++i) negm[i] = -m; }
    PIN(negm);
    f32x16 sa = MFMA32(ldsfrag(kp), q0, negm), sb; sa = MFMA32(ldsfrag(kp + 8192), q1, sa);
    bf16x8 k0 = ldsfrag(kp + 1024), k1 = ldsfrag(kp + 8192 + 1024), v0 = ldsfrag(vp), v1 = v0;
    u32x4 p0 = {0u, 0u, 0u, 0u}, p1 = {0u, 0u, 0u, 0u};
    float t14 = 0.f, t15 = 0.f;
    o = f32x16{};
    PIN(sa); PIN(o); PIN(p0); PIN(p1); PIN(k0); PIN(k1); PIN(v0); PIN(v1);
    MFMA_DRAIN();
#define GRP(wprev, wcur, c0, c1, a0, a1, a2, a3) do { float t0_, t1_, n0_, n1_; \
        asm volatile("v_exp_f32 %2, %6\n\tv_exp_f32 %3, %7\n\tv_cvt_pk_bf16_f32 %0, %10, %11\n\tv_exp_f32 %4, %8\n\tv_exp_f32 %5, %9\n\tv_cvt_pk_bf16_f32 %1, %2, %3" \
            : "=&v"(wprev), "=&v"(wcur), "=&v"(t0_), "=&v"(t1_), "=&v"(n0_), "=&v"(n1_) : "v"(a0), "v"(a1), "v"(a2), "v"(a3), "v"(c0), "v"(c1)); \
        c0 = n0_; c1 = n1_; } while (0)
    lds_cptr kp2 = kp + 2 * AT_SLOT, vp2 = vp + 2 * AT_SLOT;
    PIN(kp); PIN(vp); PIN(kp2); PIN(vp2);
    auto kaddr = [&](auto TT, int blk) { constexpr int t = TT.value, sl = (t >> 3) % AT_R; return (sl < 2 ? kp + sl * AT_SLOT : kp2) + blk * 8192 + (t & 7) * 1024; };
    auto vaddr = [&](auto KG) { constexpr int kg = KG.value, sl = (kg >> 4) % AT_R; return (sl < 2 ? vp + sl * AT_SLOT : vp2) + (kg & 15) * AT_VKG; };
    static_for<0, 64>([&](auto TT) { constexpr int t = TT.value, c = t >> 3, i = t & 7;
        if constexpr (i == 6 && c < 7) { SBAR(); hook.template wait<c>(); hook.template sync<c>(); SBAR(); }
        { constexpr int j = (i + 2) & 7, jp = (j + 7) & 7;
          constexpr int pr = j < 2 ? 3 : j < 5 ? 2 : j < 7 ? 1 : 0, pp = jp < 2 ? 3 : jp < 5 ? 2 : jp < 7 ? 1 : 0;
          if constexpr (pr != pp) { __builtin_amdgcn_s_setprio(pr); SBAR(); } }
        f32x16& sc = (t & 1) ? sb : sa; f32x16& sx = (t & 1) ? sa : sb;
        unsigned wa, wb, wc;
        if constexpr (t + 1 < 64) { sx = MFMA32(k0, q0, negm); SBAR(); }
        if constexpr (t + 2 < 64) { k0 = ldsfrag(kaddr(IC<t + 2>{}, 0)); SBAR(); }
        GRP(wa, wb, t14, t15, sc[0], sc[1], sc[2], sc[3]); p1[3] = wa; p0[0] = wb; SBAR();
        o = MFMA32(v1, __builtin_bit_cast(bf16x8, p1), o); SBAR();
        v1 = ldsfrag(vaddr(IC<2 * t + 1>{})); SBAR();
        GRP(wa, wb, t14, t15, sc[4], sc[5], sc[6], sc[7]); p0[1] = wa; p0[2] = wb; SBAR();
        if constexpr (i >= 6) { if constexpr (c + 2 < 8) { issue.template part<i - 6>(c + 2, wid); SBAR(); } }
        else if constexpr (i < 2) { if constexpr (c >= 1 && c + 1 < 8) { issue.template part<i + 2>(c + 1, wid); SBAR(); } }
        else { hook.template tile<c, i>(); SBAR(); }
        if constexpr (t + 1 < 64) { sx = MFMA32(k1, q1, sx); SBAR(); }
        if constexpr (t + 2 < 64) { k1 = ldsfrag(kaddr(IC<t + 2>{}, 1)); SBAR(); }
        GRP(wa, wc, t14, t15, sc[8], sc[9], sc[10], sc[11]); p0[3] = wa; SBAR();
        o = MFMA32(v0, __builtin_bit_cast(bf16x8, p0), o); SBAR();
        p1[0] = wc;
        if constexpr (t + 1 < 64) { v0 = ldsfrag(vaddr(IC<2 * t + 2>{})); SBAR(); }
        GRP(wa, wb, t14, t15, sc[12], sc[13], sc[14], sc[15]); p1[1] = wa; p1[2] = wb; SBAR();
    });
#undef GRP
    __builtin_amdgcn_s_setprio(0);
    p1[3] = cvtpk(t14, t15);
    o = MFMA32(v1, __builtin_bit_cast(bf16x8, p1), o);
}
DI void attn_safe(const AttnIssue& issue, int wid, lds_cptr kp, lds_cptr vp, const bf16x8 q0, const bf16x8 q1, f32x16& o) {
    issue.all(0, wid); issue.all(1, wid);
    o = f32x16{};
    float m_run = -INFINITY;
    for (int c = 0; c < 8; ++c) {
        if (c == 7) wait_vm_bar<0>(); else wait_vm_bar<4>();
        if (c + 2 < 8) issue.all(c + 2, wid);
        const int so = (c % AT_R) * AT_SLOT;
        for (int i = 0; i < 8; ++i) {
            f32x16 s = MFMA32(ldsfrag(kp + so + i * 1024), q0, f32x16{}); s = MFMA32(ldsfrag(kp + so + 8192 + i * 1024), q1, s);
            float m = s[0];
#pragma unroll
            for (int k = 1; k < 16; ++k) m = fmaxf(m, s[k]);
            m = fmaxf(half_max(m), m_run);
            const float alpha = __builtin_amdgcn_exp2f(m_run - m);
            m_run = m;
#pragma unroll
            for (int k = 0; k < 16; ++k) o[k] *= alpha;
            u32x4 p0, p1;
#pragma unroll
            for (int k = 0; k < 4; ++k) { p0[k] = cvtpk(__builtin_amdgcn_exp2f(s[2 * k] - m), __builtin_amdgcn_exp2f(s[2 * k + 1] - m));
                                          p1[k] = cvtpk(__builtin_amdgcn_exp2f(s[8 + 2 * k] - m), __builtin_amdgcn_exp2f(s[8 + 2 * k + 1] - m)); }
            o = MFMA32(ldsfrag(vp + so + (2 * i) * AT_VKG), __builtin_bit_cast(bf16x8, p0), o);
            o = MFMA32(ldsfrag(vp + so + (2 * i + 1) * AT_VKG), __builtin_bit_cast(bf16x8, p1), o);
        }
    }
}

struct AttnState { AttnIssue issue; u32x4 qa, qb; };
DI void attn_setup(AttnState& st, char* ws, char* lds, int hd) {
    const int tid = threadIdx.x, lane = tid & 63, wid = __builtin_amdgcn_readfirstlane(tid >> 6);
    const int b0 = (hd * 18) >> 4, vr = lane < 21 ? lane : lane - 21;
    st.issue.kb0 = uni(ws + KF_OFF + (size_t)b0 * 65536 + wid * 1024); st.issue.vb0 = uni(ws + VT_OFF + (size_t)hd * 128 * 576 + wid * 2 * 576);
    st.issue.koff = lane * 16; st.issue.voff = (lane < 21 ? 0 : 288) + vr * 16; st.issue.vmask = __ballot(lane < 39 && vr < 18);
    st.issue.lds0 = __builtin_amdgcn_readfirstlane((unsigned)(uintptr_t)lds + wid * 1024);
    if (tid < AT_R * 16 * 2 * 3) { const int i = tid, row = i % 3, hh = (i / 3) & 1, kg = (i / 6) & 15, sl = i / 96;
        const unsigned v = row == 0 ? 0x3f803f80u : 0u;
        *(u32x4*)(lds + sl * AT_SLOT + AT_K + kg * AT_VKG + hh * 336 + (18 + row) * 16) = (u32x4){v, v, v, v}; }
    if (tid == 0) *(volatile int*)(lds + MISC) = 0;
}
template <int N> DI void attn_request(AttnState& st, char* ws, int hd, int qb) {
    const int wid = __builtin_amdgcn_readfirstlane(threadIdx.x >> 6);
    if constexpr (N < 2) { const int b0 = (hd * 18) >> 4, T = qb * 8 + wid;
        gload16_ho(N == 0 ? st.qa : st.qb, st.issue.koff, uni(ws + QF_OFF + ((size_t)(b0 + N) * 64 + T) * 1024)); }
    else st.issue.template part<(N - 2) & 3>((N - 2) >> 2, wid);
}
template <typename Hook, typename Late>
DI void attn_body(AttnState& st, char* ws, char* lds, int hd, int qb, bool pre, Hook& hook, Late& late) {
    const int tid = threadIdx.x, lane = tid & 63, wid = __builtin_amdgcn_readfirstlane(tid >> 6), h = lane >> 5;
    const int off = (hd * 18) & 15, T = qb * 8 + wid;
    if (pre) wait_vm<14>(); else wait_vm<8>();
    PIN(st.qa); PIN(st.qb);
    u32x4 qa = st.qa, qb_ = st.qb;
#pragma unroll
    for (int k = 0; k < 4; ++k) { const int c = 8 * h + 2 * k; if (c < off) qa[k] = 0u; if (c >= off + 2) qb_[k] = 0u; }
    const bf16x8 q0 = __builtin_bit_cast(bf16x8, qa), q1 = __builtin_bit_cast(bf16x8, qb_);
    const int r32 = lane & 31, reff = r32 < 19 ? r32 : ((r32 >= 20 && r32 <= 27) ? 20 : 19);
    const lds_cptr kp = (lds_cptr)lds + lane * 16, vp = (lds_cptr)lds + AT_K + h * 336 + reff * 16;
    f32x16 o;
    STAMP(10);
    attn_fast(st.issue, wid, kp, vp, q0, q1, o, pre, hook);
    STAMP(11);
    { float l = o[10]; auto sw = __builtin_amdgcn_permlane32_swap(__float_as_uint(l), __float_as_uint(l), false, false); l = __uint_as_float(sw[0]);
      float mx = fabsf(o[0]);
#pragma unroll
      for (int k = 1; k < 10; ++k) mx = fmaxf(mx, fabsf(o[k]));
      const bool bad = !(l < 1.0e30f) || !(mx < 1.0e37f) || !(l > 1.0e-30f);
      if (__any(bad) && lane == 0) *(volatile int*)(lds + MISC) = 1; }
    BAR();
    if (*(volatile int*)(lds + MISC)) { BAR(); attn_safe(st.issue, wid, kp, vp, q0, q1, o); BAR(); }
    late();
    float l = o[10]; { auto sw = __builtin_amdgcn_permlane32_swap(__float_as_uint(l), __float_as_uint(l), false, false); l = __uint_as_float(sw[0]); }
    const float inv = 1.0f / l;
    u32x4 ca;
#pragma unroll
    for (int k = 0; k < 4; ++k) ca[k] = cvtpk(o[2 * k] * inv, o[2 * k + 1] * inv);
    char* ob = uni(ws + OF_OFF + ((size_t)hd * 128 + 2 * T) * 512);
    gstore16_ho(((lane >> 4) & 1) * 512 + h * 256 + (lane & 15) * 16, ob, ca);
    char* tb = uni(ws + OT_OFF + (size_t)(2 * T) * 2048 + (hd >> 4) * 1024 + ((hd >> 2) & 3) * 256 + (hd & 3) * 4);
    const unsigned ct = cvtpk(o[8] * inv, o[9] * inv), to = ((lane >> 4) & 1) * 2048 + (lane & 15) * 16;
    asm volatile("s_mov_b64 exec, %3\n\tglobal_store_dword %0, %1, %2 sc1\n\ts_mov_b64 exec, -1" :: "v"(to), "v"(ct), "s"(tb), "s"(0xffffffffull) : "memory");
}

constexpr int CD_PD = 8;
struct CdLoader {
    unsigned voff[5], d0; const char* w2b; const char* wob;
    template <int KIND> DI void offsets(int hw, int lane) {
#pragma unroll
        for (int i = 0; i < 5; ++i) { const int ci = 288 * hw + 64 * i + lane, row = (ci * 3641) >> 16, c = ci - row * 18;
            const int gr = KIND == 0 ? row : KIND == 1 ? 18 * (row >> 4) + (row & 15) : 18 * (row >> 1) + 16 + (row & 1);
            voff[i] = gr * 1152 + c * 16; } }
    DI void init(const Params& P, unsigned lds0, int hw, int lane, int ng, int ks) {
        offsets<0>(hw, lane);
        w2b = uni((const char*)P.w2 + (size_t)ks * 1152 * 1152 + ng * 288); wob = uni((const char*)P.wo + ng * 288);
        d0 = __builtin_amdgcn_readfirstlane(lds0 + CD_RING + hw * 288 * 16); }
    template <int I> DI void one(int t) const {
        const unsigned d = d0 + ((t + 1) % 5) * CD_STAGE; const char* sb = t < 18 ? w2b + (size_t)t * 64 * 1152 : t < 26 ? wob + (size_t)(t - 18) * 72 * 1152 : wob;
        if constexpr (I < 4) glds_in(voff[I], sb, d + I * 1024); else glds_in_m(voff[4], sb, d + 4096, 0xffffffffull); }
    DI void operator()(int t) const { one<0>(t); one<1>(t); one<2>(t); one<3>(t); one<4>(t); }
};
struct HFrags { bf16x8 f[CD_PD][2]; };
template <int S> DI void h_issue(HFrags& H, const char* hb, unsigned hoff) { gload16_ho(H.f[S % CD_PD][0], hoff, hb + S * 1024); gload16_ho(H.f[S % CD_PD][1], hoff, hb + (144 + S) * 1024); }
DI const char* h_base(char* ws, int rb, int ks, int wid) { return uni(ws + HF_OFF + ((size_t)(rb * 8 + 2 * wid) * 144 + ks * 36) * 1024); }

DI void cd_body(const Params& P, char* lds, int rb, int ng, int ks, int oflag, u64 tag, HFrags& H, bool wpre) {
    char* ws = P.ws;
    const int tid = threadIdx.x, lane = tid & 63, wid = __builtin_amdgcn_readfirstlane(tid >> 6);
    const unsigned lds0 = (unsigned)(uintptr_t)lds;
    const int u0 = rb * 8 + 2 * ks;
    CdLoader ld;
    f32x4 acc[9][2];
    if (wid >= 4) {
        const int hw = wid - 4;
        ld.init(P, lds0, hw, lane, ng, ks);
        if (!wpre) { ld(0); ld(1); ld(2); }
        for (int t = 0; t < 18; ++t) {
            wait_vm<10>(); BAR0();
            if (t == 4 && tid == 256) flag_set(ws, FL_O, oflag, tag);
            if (t == 15) ld.offsets<1>(hw, lane);
            ld(t + 3);
            if (t == 9) {
                flag_wait(ws, FL_O, (rb >> 1) * 32 + lane, lane < 32, tag);
                const unsigned oo = (lane >> 5) * 65536 + (lane & 31) * 16;
                const char* ob = uni(ws + OF_OFF + (size_t)u0 * 512 + (size_t)(8 * hw) * 65536);
                const unsigned od = __builtin_amdgcn_readfirstlane(lds0 + CD_O + hw * 8192);
                static_for<0, 8>([&](auto I) { constexpr int i = I.value; glds_ho(oo, ob + (size_t)(i >> 1) * 131072 + (i & 1) * 512, od + i * 1024); });
                glds_ho(lane * 16, uni(ws + OT_OFF + (size_t)(u0 + (hw & 1)) * 2048 + (hw >> 1) * 1024), __builtin_amdgcn_readfirstlane(lds0 + CD_O + 32768 + hw * 1024));
            }
        }
    } else {
        constexpr int LAG = 3, PD = CD_PD, NALL = 36 * 9;
        const char* hb = h_base(ws, rb, ks, wid);
        const unsigned hoff = lane * 16;
        bf16x8 wf[LAG + 1];
        lds_cptr wp0 = (lds_cptr)lds + CD_RING + ((lane >> 4) * 4 + ((lane & 15) >> 2)) * CD_ROW + (lane & 3) * 8, wp2 = wp0 + 2 * CD_STAGE, wp4 = wp0 + 4 * CD_STAGE;
        PIN(wp0); PIN(wp2); PIN(wp4);
        auto frag_ptr = [&](auto I) { constexpr int i = I.value, s = i / 9, n = i % 9, sl = ((s >> 1) + 1) % 5;
            return (sl < 2 ? wp0 : sl < 4 ? wp2 : wp4) + (sl & 1) * CD_STAGE * (sl < 4 ? 1 : 0) + (s & 1) * 32 * CD_ROW + n * 32; };
        BAR0();
        STAMP(14);
        static_for<0, LAG>([&](auto I) { wf[I.value] = trfrag(frag_ptr(I), 16 * CD_ROW); });
        static_for<0, NALL>([&](auto I) {
            constexpr int i = I.value, j = i + LAG, s = i / 9, n = i % 9;
            if constexpr (j < NALL) { if constexpr (j % 18 == 0) { SBAR(); BAR0(); SBAR(); } wf[j % (LAG + 1)] = trfrag(frag_ptr(IC<j>{}), 16 * CD_ROW); }
            if constexpr (n == 0) {
                constexpr int last = s + PD - 2 < 36 ? s + PD - 2 : 35;
                wait_vm<2 * (last - s)>();
                PIN(H.f[s % PD][0]); PIN(H.f[s % PD][1]);
                if constexpr (s + PD - 1 < 36) h_issue<s + PD - 1>(H, hb, hoff);
                SBAR();
            }
            const bf16x8 w = wf[i % (LAG + 1)];
            if constexpr (s == 0) { mfma16_zero(acc[n][0], w, H.f[s % PD][0]); mfma16_zero(acc[n][1], w, H.f[s % PD][1]); }
            else { mfma16_acc(acc[n][0], w, H.f[s % PD][0]); mfma16_acc(acc[n][1], w, H.f[s % PD][1]); }
            SBAR();
        });
        STAMP(15);
        MFMA_DRAIN();
        static_for<0, 9>([&](auto N) { PIN(acc[N.value][0]); PIN(acc[N.value][1]); });
    }
    STAMP(16);
    const int g = lane >> 4;
    char* gpb = ws + GP_OFF + (size_t)(rb * 4 + ng) * 16 * 9 * 1024;
    auto tick_h = [&](auto KK) { constexpr int k = KK.value;
        if constexpr (k <= 6) wait_vm<10>(); else if constexpr (k == 7) wait_vm<5>(); else wait_vm<0>();
        BAR0();
        if constexpr (k == 5) ld.offsets<2>(wid - 4, lane);
        if constexpr (k + 3 < 9) ld(18 + k + 3); };
    if (wid >= 4) {
        const int hw = wid - 4;
        static_for<0, 3>([&](auto K) { tick_h(K); });
        if (tid == 256) flag_set(ws, FL_G, (rb * 4 + ng) * 4 + ks, tag);
        static_for<3, 9>([&](auto K) { tick_h(K); });
        flag_wait(ws, FL_G, (rb * 4 + ng) * 4 + lane, lane < 4, tag);
        u32x4 gp[3][3]; u32x2 bb[3];
        static_for<0, 3>([&](auto UU) { constexpr int un = UU.value;
            if (un < 2 || hw == 0) { const int nt = hw + 4 * un;
                static_for<0, 3>([&](auto K) { constexpr int kk = K.value; const int src = kk + (kk >= ks ? 1 : 0);
                    gload16_ho(gp[un][kk], lane * 16, uni(gpb + ((ks * 4 + src) * 9 + nt) * 1024)); });
                gload8_in(bb[un], (unsigned)((ng * 144 + nt * 16 + g * 4) * 2), P.b2); } });
        wait_vm<0>();
        static_for<0, 3>([&](auto UU) { constexpr int un = UU.value;
            if (un < 2 || hw == 0) { const int nt = hw + 4 * un;
                PIN(gp[un][0]); PIN(gp[un][1]); PIN(gp[un][2]); PIN(bb[un]);
                const float bv[4] = {bf_lo(bb[un][0]), bf_hi(bb[un][0]), bf_lo(bb[un][1]), bf_hi(bb[un][1])};
#pragma unroll
                for (int u = 0; u < 2; ++u) { f32x4* gp_l = (f32x4*)(lds + CD_OWN + (2 * nt + u) * 1024 + lane * 16); f32x4 v = *gp_l;
#pragma unroll
                    for (int k = 0; k < 3; ++k) { v[0] += bf_lo(gp[un][k][2 * u]); v[1] += bf_hi(gp[un][k][2 * u]); v[2] += bf_lo(gp[un][k][2 * u + 1]); v[3] += bf_hi(gp[un][k][2 * u + 1]); }
#pragma unroll
                    for (int i = 0; i < 4; ++i) { const float gg = v[i] + bv[i]; v[i] = gg * __builtin_amdgcn_rcpf(1.0f + __expf(-gg)); }
                    *gp_l = v; } } });
        BAR();
    } else {
        const int qq = (lane & 15) >> 2, p = lane & 3;
        lds_cptr wm = (lds_cptr)lds + CD_RING + (16 * (g >> 1) + 4 * (g & 1) + qq) * CD_ROW + p * 8 + wid * 32, wt = (lds_cptr)lds + CD_RING + (8 * g + qq) * CD_ROW + p * 8 + wid * 32;
        lds_cptr op = (lds_cptr)lds + CD_O + lane * 16;
        lds_cptr wm2 = wm + 2 * CD_STAGE, wm4 = wm + 4 * CD_STAGE;
        PIN(wm); PIN(wm2); PIN(wm4); PIN(wt); PIN(op);
        f32x4 sacc[3][2];
        struct Step { bf16x8 o0, o1, w[3]; };
        constexpr int LA = 2;
        Step stp[LA + 1];
        auto rd = [&](auto KS) { constexpr int s = KS.value, k = s >> 1, sl = (k + 4) % 5;
            Step& st = stp[s % (LA + 1)];
            st.o0 = ldsfrag(op + s * 2048); st.o1 = ldsfrag(op + s * 2048 + 1024);
            static_for<0, 3>([&](auto N) { constexpr int n = N.value;
                const int dn = (n < 2 || wid == 0 ? n : 1) * 128;
                if constexpr (k < 8) { const lds_cptr a = (sl < 2 ? wm : sl < 4 ? wm2 : wm4) + (sl < 4 ? (sl & 1) * CD_STAGE : 0) + (s & 1) * 32 * CD_ROW + dn; st.w[n] = trfrag(a, 8 * CD_ROW); }
                else { const lds_cptr a = wt + sl * CD_STAGE + (s & 1) * 32 * CD_ROW + dn; st.w[n] = trfrag(a, 4 * CD_ROW); } }); };
        auto mm = [&](auto KS) { constexpr int s = KS.value; Step& st = stp[s % (LA + 1)];
            static_for<0, 2>([&](auto N) { constexpr int n = N.value;
                if constexpr (s == 0) { mfma16_zero(sacc[n][0], st.w[n], st.o0); mfma16_zero(sacc[n][1], st.w[n], st.o1); } else { mfma16_acc(sacc[n][0], st.w[n], st.o0); mfma16_acc(sacc[n][1], st.w[n], st.o1); } });
            if (wid == 0) { if constexpr (s == 0) { mfma16_zero(sacc[2][0], st.w[2], st.o0); mfma16_zero(sacc[2][1], st.w[2], st.o1); } else { mfma16_acc(sacc[2][0], st.w[2], st.o0); mfma16_acc(sacc[2][1], st.w[2], st.o1); } } };
        char* gb = uni(gpb + (wid * 4 + ks) * 9 * 1024);
        auto spill = [&](auto NN) { constexpr int n = NN.value;
            if (wid == ks) { *(f32x4*)(lds + CD_OWN + (2 * n) * 1024 + lane * 16) = acc[n][0]; *(f32x4*)(lds + CD_OWN + (2 * n + 1) * 1024 + lane * 16) = acc[n][1]; }
            else gstore16_ho(lane * 16, gb + n * 1024, (u32x4){cvtpk(acc[n][0][0], acc[n][0][1]), cvtpk(acc[n][0][2], acc[n][0][3]), cvtpk(acc[n][1][0], acc[n][1][1]), cvtpk(acc[n][1][2], acc[n][1][3])}); };
        const int col0 = ng * 144 + wid * 16 + g * 4, tok0 = u0 * 16 + (lane & 15);
        u32x2 xx[3][2];
        auto tick = [&](auto KK) { constexpr int k = KK.value; SBAR(); if constexpr (k == 2) wait_vm<0>(); BAR(); STAMP(21 + k);
            if constexpr (k == 4) static_for<0, 3>([&](auto N) { constexpr int n = N.value; if (n < 2 || wid == 0) {
                gload8_in(xx[n][0], (unsigned)((tok0 * DM + col0 + n * 64) * 2), P.x); gload8_in(xx[n][1], (unsigned)(((tok0 + 16) * DM + col0 + n * 64) * 2), P.x); } });
            SBAR(); };
        static_for<0, 9>([&](auto N) { spill(N); });
        tick(IC<0>{});
        static_for<0, LA>([&](auto S) { rd(S); });
        static_for<0, 18>([&](auto S) { constexpr int s = S.value;
            if constexpr (s + LA < 18) { if constexpr (((s + LA) & 1) == 0) tick(IC<(s + LA) / 2>{}); rd(IC<s + LA>{}); }
            SBAR();
            mm(S);
            SBAR();
        });
        STAMP(18);
        MFMA_DRAIN();
        wait_vm<0>();
        static_for<0, 3>([&](auto N) { PIN(sacc[N.value][0]); PIN(sacc[N.value][1]); PIN(xx[N.value][0]); PIN(xx[N.value][1]); });
        BAR();
        STAMP(19);
        static_for<0, 3>([&](auto N) { constexpr int n = N.value; if (n < 2 || wid == 0) {
#pragma unroll
            for (int u = 0; u < 2; ++u) { const int nt = wid + 4 * n; const f32x4 gt = *(const f32x4*)(lds + CD_OWN + (2 * nt + u) * 1024 + lane * 16);
                const float o0 = bf_lo(xx[n][u][0]) + sacc[n][u][0] * gt[0], o1 = bf_hi(xx[n][u][0]) + sacc[n][u][1] * gt[1], o2 = bf_lo(xx[n][u][1]) + sacc[n][u][2] * gt[2], o3 = bf_hi(xx[n][u][1]) + sacc[n][u][3] * gt[3];
                *(u32x2*)(P.out + (size_t)(tok0 + u * 16) * DM + col0 + n * 64) = (u32x2){cvtpk(o0, o1), cvtpk(o2, o3)}; } } });
    }
}

DI int qkv_flag_index(int hd, int qb) {
    int l2 = threadIdx.x & 63; asm volatile("" : "+v"(l2));
    const int b0 = (hd * 18) >> 4, kind = l2 >> 4, i = l2 & 15, rr = kind == 0 ? qb : (i >> 1);
    const int tqk = (kind == 1 ? 36 : 0) + b0 + (i & 1), tv = (hd * 18 + 17 * (i & 1)) >> 4;
    const int cgq = tqk < 48 ? 2 * (tqk / 3) : tqk < 60 ? 2 * (9 + (tqk - 48) / 4) + 1 : 2 * (12 + (tqk - 60) / 3) + 1;
    return rr * 32 + (kind >= 2 ? 2 * (tv >> 2) + 1 : cgq); }
struct HookA { char* ws; char* lds; int wid, lane, hd, qb; u64 tag; AttnState& ast; bool& pre; u32x2 fv;
    DI void ask() { if (wid == 0) { const int i = qkv_flag_index(hd, qb); asm volatile("global_load_dwordx2 %0, %1, %2 sc1" : "=v"(fv) : "v"(i * 8), "s"(ws + FL_QKV) : "memory"); } }
    DI void check() { if (wid == 0) { wait_vm<0>(); PIN(fv); const bool ok = __all(lane >= 48 || (fv[0] == (unsigned)tag && fv[1] == (unsigned)(tag >> 32)));
                                      if (lane == 0) *(volatile int*)(lds + MISC + 16) = ok ? 1 : 0; } }
    DI void go() { BAR(); pre = __builtin_amdgcn_readfirstlane(*(volatile int*)(lds + MISC + 16)) != 0; if (pre) attn_setup(ast, ws, lds, hd); }
    template <int N> DI void step() { if (pre) attn_request<N>(ast, ws, hd, qb); }
};
struct HookB { const Params& P; char* ws; char* lds; unsigned lds0; int tid, lane, wid, j, aflag, rbC, ng, ks; u64 tag; CdLoader ld;
    template <int C> DI void wait() { if constexpr (C >= 3 && C <= 5) { if (wid >= 4) wait_vm<5>(); else wait_vm<0>(); BAR(); } else wait_vm_bar<0>(); }
    template <int C> DI void sync() {
        if constexpr (C == 0) { if (tid == 0) flag_set(ws, FL_H, aflag, tag); }
        if constexpr (C == 1) {
            const int idx = tid; const bool isw2 = idx < 324; const int line = isw2 ? j * 324 + idx : j * 162 + (idx - 324), row = (line * 7282) >> 16, cl = line - row * 9;
            const char* a = (isw2 ? (const char*)P.w2 + (size_t)ks * 1152 * 1152 : (const char*)P.wo) + (size_t)row * 1152 + cl * 128;
            touch(a, lds0 + MISC + 512, __ballot(idx < 486)); }
        if constexpr (C == 2) { if (wid >= 4) ld.init(P, lds0, wid - 4, lane, ng, ks); }
        if constexpr (C == 3) { if (wid == 0) flag_to_lds(lane * 4, ws + FL_H + ((rbC >> 1) * 32 + 8 * ks) * 8, lds0 + MISC + 64, 0xffffull); }
        if constexpr (C == 4) { if (wid == 0) { const unsigned v = *(volatile unsigned*)(lds + MISC + 64 + (lane & 15) * 4);
                                                const bool ok = __all(v == ((lane & 1) ? (unsigned)(tag >> 32) : (unsigned)tag)); if (lane == 0) *(volatile int*)(lds + MISC + 20) = ok ? 1 : 0; } } }
    template <int C, int I> DI void tile() { if constexpr (C >= 3 && C <= 5) { if (wid >= 4) { ld.template one<I - 2>(C - 3); if constexpr (I == 5) ld.template one<4>(C - 3); } } }
};

__global__ __launch_bounds__(512, 2) void fused_block(Params P) {
    extern __shared__ __attribute__((aligned(16))) char lds[];
    char* ws = P.ws;
    const int tid = threadIdx.x, lane = tid & 63, wid = __builtin_amdgcn_readfirstlane(tid >> 6);
    const int b = blockIdx.x, xcd = b & 7, j = b >> 3;
    const unsigned lds0 = (unsigned)(uintptr_t)lds;
    STAMP(0);
    u32x2 xa[2], xb[2]; u64 ep = 0;
    { const char* xr = uni((const char*)P.x + (size_t)b * 8 * 1152);
      const int oc = (lane & 7) + 8 * wid; const unsigned vo = (lane >> 3) * 1152 + (32 * (oc >> 2) + 4 * (oc & 3)) * 2;
      gload8_in(xa[0], vo, xr); gload8_in(xb[0], vo + 32, xr);
      const int oc2 = 64 + (lane & 7); const unsigned vo2 = (lane >> 3) * 1152 + (32 * (oc2 >> 2) + 4 * (oc2 & 3)) * 2;
      gload8_in(xa[1], vo2, xr); gload8_in(xb[1], vo2 + 32, xr); }
    const int rbA = j & 7, cg = xcd * 4 + (j >> 3);
    const GaRole R = ga_role(cg);
    if (wid == 0) {
        ep = __hip_atomic_load((GAS u64*)(ws + FL_OFF), __ATOMIC_RELAXED, __HIP_MEMORY_SCOPE_AGENT);
        wait_vm<0>();
    } else {
        const int idx = 64 * (wid - 1) + lane, row = 72 * rbA + (idx * 10923 >> 16), k = idx - (idx * 10923 >> 16) * 6;
        const int lq = R.nq * 32 - 4, lf = (R.nf1 + 6) * 32 - 4;
        const char* a = k < 2 ? (const char*)P.wqkv + (size_t)row * 3456 + R.tq0 * 32 + (k ? lq : 0)
                              : (const char*)P.w1 + (size_t)row * 9216 + R.tf0 * 32 + (k == 5 ? lf : (k - 2) * 128 < lf ? (k - 2) * 128 : lf);
        touch(a, lds0 + MISC + 512, __ballot(idx < 432));
        wait_vm<1>();
    }
    PIN(xa[0]); PIN(xb[0]); PIN(xa[1]); PIN(xb[1]);
    STAMP(1);
    ln_compute(lds, wid, lane, xa, xb);
    if (wid == 0) {
        const int r = lane >> 3, c = lane & 7, row = b * 8 + r;
        char* dst = uni(ws + XN_OFF + (size_t)(b >> 1) * 18 * 1024);
        static_for<0, 9>([&](auto I) { constexpr int i = I.value; const int oc = c + 8 * i;
            gstore16_ho((oc >> 2) * 1024 + (oc & 3) * 256 + (row & 15) * 16, dst, *(const u32x4*)(lds + LN_STAGE + i * 1024 + lane * 16)); });
    }
    BAR0();
    if (wid == 0) {
        const u64 tag = (((u64)__builtin_amdgcn_readfirstlane((unsigned)(ep >> 32)) << 32) | (unsigned)__builtin_amdgcn_readfirstlane((unsigned)ep)) + 1;
        wait_vm<0>();
        if (lane == 0) { flag_set(ws, FL_LN, b, tag); *(volatile u64*)(lds + LN_PART + 512) = tag; }
        STAMP(2);
        flag_wait(ws, FL_LN, rbA * 32 + lane, lane < 32, tag);
    } else ga_prefetch(P, lds0, wid, lane, R);
    wait_vm_bar<0>();
    STAMP(3);
    const u64 tg = *(volatile u64*)(lds + LN_PART + 512);
    const u64 tag = ((u64)__builtin_amdgcn_readfirstlane((unsigned)(tg >> 32)) << 32) | (unsigned)__builtin_amdgcn_readfirstlane((unsigned)tg);
    const int aflag = rbA * 32 + cg, hd = xcd * 4 + (j & 3), qb = j >> 2;
    AttnState ast; bool pre = false;
    HookA hookA{ws, lds, wid, lane, hd, qb, tag, ast, pre, {0u, 0u}};
    if ((cg & 1) == 0)       gemmA_body<3, 4, false>(P, lds, rbA, R.tq0, R.tf0, aflag, tag, hookA);
    else if ((cg >> 1) < 9)  gemmA_body<4, 2, true>(P, lds, rbA, R.tq0, R.tf0, aflag, tag, hookA);
    else if ((cg >> 1) < 12) gemmA_body<4, 2, false>(P, lds, rbA, R.tq0, R.tf0, aflag, tag, hookA);
    else                     gemmA_body<3, 2, false>(P, lds, rbA, R.tq0, R.tf0, aflag, tag, hookA);
    STAMP(8);
    if (!pre) {
        if (wid == 0) flag_wait(ws, FL_QKV, qkv_flag_index(hd, qb), lane < 48, tag);
        BAR();
        attn_setup(ast, ws, lds, hd);
        static_for<0, 10>([&](auto N) { attn_request<N.value>(ast, ws, hd, qb); });
    }
    if (b == 0 && tid == 0) __hip_atomic_store((GAS u64*)(ws + FL_OFF), tag, __ATOMIC_RELAXED, __HIP_MEMORY_SCOPE_AGENT);
    STAMP(9);
    const int rbC = (xcd >> 2) * 8 + (j & 7), ng = j >> 3, ks = xcd & 3;
    HookB hookB{P, ws, lds, lds0, tid, lane, wid, j, aflag, rbC, ng, ks, tag, {}};
    HFrags H; bool hpre = false;
    auto lateB = [&]() { hpre = __builtin_amdgcn_readfirstlane(*(volatile int*)(lds + MISC + 20)) != 0;
        if (hpre && wid < 4) { const char* hb = h_base(ws, rbC, ks, wid); static_for<0, CD_PD - 1>([&](auto S) { h_issue<S.value>(H, hb, lane * 16); }); } };
    attn_body(ast, ws, lds, hd, qb, pre, hookB, lateB);
    STAMP(12);
    if (!hpre) {
        if (wid == 0) flag_wait(ws, FL_H, (rbC >> 1) * 32 + 8 * ks + lane, lane < 8, tag);
        wait_vm_bar<0>();
        if (wid < 4) { const char* hb = h_base(ws, rbC, ks, wid); static_for<0, CD_PD - 1>([&](auto S) { h_issue<S.value>(H, hb, lane * 16); }); }
    }
    STAMP(13);
    cd_body(P, lds, rbC, ng, ks, qb * 32 + hd, tag, H, true);
    STAMP(20);
}

extern "C" void kernel_launch(void* const* d_in, const int* in_sizes, int n_in, void* d_out, int out_size, void* d_ws, size_t ws_size, hipStream_t stream) {
    Params P;
    P.x = (const u16*)d_in[0]; P.wqkv = (const u16*)d_in[1]; P.wo = (const u16*)d_in[2];
    P.w1 = (const u16*)d_in[4]; P.b1 = (const u16*)d_in[5]; P.w2 = (const u16*)d_in[6]; P.b2 = (const u16*)d_in[7];
    P.ws = (char*)d_ws; P.out = (u16*)d_out;
    (void)hipFuncSetAttribute((const void*)fused_block, hipFuncAttributeMaxDynamicSharedMemorySize, LDS_BYTES);
    fused_block<<<256, 512, LDS_BYTES, stream>>>(P);
}
```

```cpp
#include <hip/hip_runtime.h>
#include <stdint.h>
#include <utility>

#define DI __device__ __forceinline__
#define LAS __attribute__((address_space(3)))
#define GAS __attribute__((address_space(1)))
typedef short bf16x8 __attribute__((ext_vector_type(8)));
typedef short s16x4 __attribute__((ext_vector_type(4)));
typedef float f32x2 __attribute__((ext_vector_type(2)));
typedef float f32x4 __attribute__((ext_vector_type(4)));
typedef float f32x16 __attribute__((ext_vector_type(16)));
typedef unsigned u32x2 __attribute__((ext_vector_type(2)));
typedef unsigned u32x4 __attribute__((ext_vector_type(4)));
typedef __bf16 bf16x2_t __attribute__((ext_vector_type(2)));
typedef unsigned short u16;
typedef unsigned long long u64;

constexpr int DM = 576;
constexpr int CODE_LINES = 38;
constexpr size_t XN_OFF = 0;
constexpr size_t QF_OFF = XN_OFF + 2359296;
constexpr size_t KF_OFF = QF_OFF + 2359296;
constexpr size_t VT_OFF = KF_OFF + 2359296;
constexpr size_t HF_OFF = VT_OFF + 2359296;
constexpr size_t OF_OFF = HF_OFF + 18874368;
constexpr size_t OT_OFF = OF_OFF + 2097152;
constexpr size_t GP_OFF = OT_OFF + 262144;
constexpr size_t FL_OFF = GP_OFF + 18874368;
constexpr size_t FL_LN = FL_OFF + 4096, FL_QKV = FL_OFF + 8192, FL_H = FL_OFF + 12288, FL_O = FL_OFF + 16384, FL_G = FL_OFF + 20480;
constexpr size_t PROF_OFF = 128u << 20;
#define STAMP(I) do {} while (0)
DI unsigned cvtpk(float lo, float hi) { f32x2 v = {lo, hi}; bf16x2_t b = __builtin_convertvector(v, bf16x2_t); return __builtin_bit_cast(unsigned, b); }
DI float bf_lo(unsigned w) { return __uint_as_float(w << 16); }
DI float bf_hi(unsigned w) { return __uint_as_float(w & 0xffff0000u); }
DI float rbf(float x) { return bf_lo(cvtpk(x, x)); }
template <int V> struct IC { static constexpr int value = V; };
template <int B, typename Fn, int... Is> DI void static_for_impl(Fn& fn, std::integer_sequence<int, Is...>) { (fn(IC<B + Is>{}), ...); }
template <int B, int E, typename Fn> DI void static_for(Fn&& fn) { if constexpr (B < E) static_for_impl<B>(fn, std::make_integer_sequence<int, E - B>{}); }
#define SBAR() __builtin_amdgcn_sched_barrier(0)
template <typename T> DI void pin_(T& x) { asm volatile("" : "+v"(x)); }
#define PIN(x) pin_(x)
#define MFMA16(a, b, c) __builtin_amdgcn_mfma_f32_16x16x32_bf16((a), (b), (c), 0, 0, 0)
DI void mfma16_acc(f32x4& acc, const bf16x8& a, const bf16x8& b) { asm volatile("v_mfma_f32_16x16x32_bf16 %0, %1, %2, %0" : "+v"(acc) : "v"(a), "v"(b)); }
DI void mfma16_zero(f32x4& acc, const bf16x8& a, const bf16x8& b) { asm volatile("v_mfma_f32_16x16x32_bf16 %0, %1, %2, 0" : "=&v"(acc) : "v"(a), "v"(b)); }
DI void mfma16_init(f32x4& acc, const bf16x8& a, const bf16x8& b, const f32x4& c) { asm volatile("s_nop 1\n\tv_mfma_f32_16x16x32_bf16 %0, %1, %2, %3" : "=&v"(acc) : "v"(a), "v"(b), "v"(c)); }
#define MFMA_DRAIN() asm volatile("s_nop 7\n\ts_nop 7" ::: "memory")
#define MFMA32(a, b, c) __builtin_amdgcn_mfma_f32_32x32x16_bf16((a), (b), (c), 0, 0, 0)

DI void glds_in(unsigned voff, const void* sbase, unsigned lds_base) {
    asm volatile("s_mov_b32 m0, %2\n\ts_nop 0\n\tglobal_load_lds_dwordx4 %0, %1" :: "v"(voff), "s"(sbase), "s"(lds_base) : "memory"); }
DI void glds_in_m(unsigned voff, const void* sbase, unsigned lds_base, u64 mask) {
    asm volatile("s_mov_b32 m0, %2\n\ts_mov_b64 exec, %3\n\tglobal_load_lds_dwordx4 %0, %1\n\ts_mov_b64 exec, -1" :: "v"(voff), "s"(sbase), "s"(lds_base), "s"(mask) : "memory"); }
DI void glds_in_vm(const void* g, unsigned lds_base, u64 mask) {
    asm volatile("s_mov_b32 m0, %1\n\ts_mov_b64 exec, %2\n\tglobal_load_lds_dwordx4 %0, off\n\ts_mov_b64 exec, -1" :: "v"(g), "s"(lds_base), "s"(mask) : "memory"); }
DI void glds_ho(unsigned voff, const void* sbase, unsigned lds_base) {
    asm volatile("s_mov_b32 m0, %2\n\ts_nop 0\n\tglobal_load_lds_dwordx4 %0, %1 sc1" :: "v"(voff), "s"(sbase), "s"(lds_base) : "memory"); }
DI void glds_ho_m(unsigned voff, const void* sbase, unsigned lds_base, u64 mask) {
    asm volatile("s_mov_b32 m0, %2\n\ts_mov_b64 exec, %3\n\tglobal_load_lds_dwordx4 %0, %1 sc1\n\ts_mov_b64 exec, -1" :: "v"(voff), "s"(sbase), "s"(lds_base), "s"(mask) : "memory"); }
template <typename T> DI void gload16_ho(T& d, unsigned voff, const void* sbase) { asm volatile("global_load_dwordx4 %0, %1, %2 sc1" : "=v"(d) : "v"(voff), "s"(sbase) : "memory"); }
DI void gload8_in(u32x2& d, unsigned voff, const void* sbase) { asm volatile("global_load_dwordx2 %0, %1, %2" : "=v"(d) : "v"(voff), "s"(sbase) : "memory"); }
DI void gstore16_ho(unsigned voff, void* sbase, u32x4 v) { asm volatile("global_store_dwordx4 %0, %1, %2 sc1\n\ts_nop 1" :: "v"(voff), "v"(v), "s"(sbase) : "memory"); }
template <int N> DI void wait_vm() { asm volatile("s_waitcnt vmcnt(%0)" :: "n"(N) : "memory"); }
template <int N> DI void wait_vm_bar() { asm volatile("s_waitcnt vmcnt(%0) lgkmcnt(0)\n\ts_barrier" :: "n"(N) : "memory"); }
typedef short v4i16_t __attribute__((ext_vector_type(4)));
typedef LAS const char* lds_cptr;
DI s16x4 vtr(lds_cptr p) { return __builtin_bit_cast(s16x4, __builtin_amdgcn_ds_read_tr16_b64_v4i16((LAS v4i16_t*)p)); }
DI bf16x8 trfrag(lds_cptr p, int off2) { s16x4 lo = vtr(p), hi = vtr(p + off2); return __builtin_shufflevector(lo, hi, 0, 1, 2, 3, 4, 5, 6, 7); }
DI bf16x8 ldsfrag(lds_cptr p) { return *(const LAS bf16x8*)p; }
template <typename T> DI T* uni(T* p) {
    uint64_t a = (uint64_t)p; unsigned lo = __builtin_amdgcn_readfirstlane((unsigned)a), hi = __builtin_amdgcn_readfirstlane((unsigned)(a >> 32));
    return (T*)(((uint64_t)hi << 32) | lo); }

DI void flag_set(char* ws, size_t arr, int idx, u64 tag) { __hip_atomic_store((GAS u64*)(ws + arr) + idx, tag, __ATOMIC_RELAXED, __HIP_MEMORY_SCOPE_AGENT); }
DI void flag_wait(char* ws, size_t arr, int idx, bool act, u64 tag) {
    GAS u64* p = (GAS u64*)(ws + arr) + (act ? idx : 0);
    for (unsigned spin = 0; spin < (1u << 22); ++spin) {
        const u64 v = __hip_atomic_load(p, __ATOMIC_RELAXED, __HIP_MEMORY_SCOPE_AGENT);
        if (__all(!act || v == tag)) break;
        __builtin_amdgcn_s_sleep(1);
    }
}
DI void touch(const void* g, unsigned lds_dummy, u64 mask) {
    asm volatile("s_mov_b32 m0, %1\n\ts_mov_b64 exec, %2\n\tglobal_load_lds_dword %0, off\n\ts_mov_b64 exec, -1" :: "v"(g), "s"(lds_dummy), "s"(mask) : "memory"); }
DI void flag_to_lds(unsigned voff, const void* sbase, unsigned lds_dst, u64 mask) {
    asm volatile("s_mov_b32 m0, %2\n\ts_mov_b64 exec, %3\n\tglobal_load_lds_dword %0, %1 sc1\n\ts_mov_b64 exec, -1" :: "v"(voff), "s"(sbase), "s"(lds_dst), "s"(mask) : "memory"); }
DI void flag_load(u32x2& d, const char* ws, size_t arr, int idx) { asm volatile("global_load_dwordx2 %0, %1, %2 sc1" : "=v"(d) : "v"(idx * 8), "s"(ws + arr) : "memory"); }
DI bool flag_all(const u32x2& v, bool act, u64 tag) { return __all(!act || (v[0] == (unsigned)tag && v[1] == (unsigned)(tag >> 32))); }
#define BAR0() asm volatile("s_barrier" ::: "memory")
#define BAR() asm volatile("s_waitcnt lgkmcnt(0)\n\ts_barrier" ::: "memory")

struct Params { const u16* x; const u16* wqkv; const u16* wo; const u16* w1; const u16* b1; const u16* w2; const u16* b2; char* ws; u16* out; };

constexpr int GA_ROW = 224, GA_SLOT = 192 * GA_ROW, GA_BIAS = 3 * GA_SLOT;
constexpr int LN_PART = GA_BIAS + 1024, LN_STAGE = LN_PART + 1024;
constexpr int AT_K = 16384, AT_VKG = 672, AT_SLOT = AT_K + 16 * AT_VKG, AT_R = 3;
constexpr int CD_O = 0, CD_OWN = 36864, CD_RING = 67584, CD_ROW = 288, CD_STAGE = 64 * CD_ROW;
constexpr int MISC = 160000, LDS_BYTES = 163840;
constexpr float QSCALE = 0.23570226039551584f * 1.4426950408889634f;

template <int CTRL> DI float dpp_add(float v) { return v + __int_as_float(__builtin_amdgcn_update_dpp(0, __float_as_int(v), CTRL, 0xf, 0xf, true)); }
DI float sum8(float v) { v = dpp_add<0xB1>(v); v = dpp_add<0x4E>(v); return dpp_add<0x141>(v); }
DI void ln_compute(char* lds, int wid, int lane, const u32x2 (&a)[2], const u32x2 (&b)[2]) {
    const int r = lane >> 3, c = lane & 7; const bool two = wid == 1;
    float v[2][8];
#pragma unroll
    for (int i = 0; i < 2; ++i) { v[i][0] = bf_lo(a[i][0]); v[i][1] = bf_hi(a[i][0]); v[i][2] = bf_lo(a[i][1]); v[i][3] = bf_hi(a[i][1]);
                                  v[i][4] = bf_lo(b[i][0]); v[i][5] = bf_hi(b[i][0]); v[i][6] = bf_lo(b[i][1]); v[i][7] = bf_hi(b[i][1]); }
    float* part = (float*)(lds + LN_PART);
    float s = ((v[0][0] + v[0][1]) + (v[0][2] + v[0][3])) + ((v[0][4] + v[0][5]) + (v[0][6] + v[0][7]));
    if (two) s += ((v[1][0] + v[1][1]) + (v[1][2] + v[1][3])) + ((v[1][4] + v[1][5]) + (v[1][6] + v[1][7]));
    s = sum8(s);
    if (c == 0) part[r * 8 + wid] = s;
    BAR();
    const f32x4 p0 = *(const f32x4*)(part + r * 8), p1 = *(const f32x4*)(part + r * 8 + 4);
    const float mean = (((p0[0] + p0[1]) + (p0[2] + p0[3])) + ((p1[0] + p1[1]) + (p1[2] + p1[3]))) * (1.0f / DM);
    float q = 0.f;
#pragma unroll
    for (int k = 0; k < 8; ++k) { const float d = v[0][k] - mean; q += d * d; }
    if (two) {
#pragma unroll
        for (int k = 0; k < 8; ++k) { const float d = v[1][k] - mean; q += d * d; } }
    q = sum8(q);
    if (c == 0) part[64 + r * 8 + wid] = q;
    BAR();
    const f32x4 q0 = *(const f32x4*)(part + 64 + r * 8), q1 = *(const f32x4*)(part + 64 + r * 8 + 4);
    const float var = (((q0[0] + q0[1]) + (q0[2] + q0[3])) + ((q1[0] + q1[1]) + (q1[2] + q1[3]))) * (1.0f / DM);
    const float m_bf = rbf(mean), den = rbf(sqrtf(rbf(rbf(var) + 1e-5f))), inv = 1.0f / den;
#pragma unroll
    for (int i = 0; i < 2; ++i) if (i == 0 || two) { u32x4 o;
#pragma unroll
        for (int k = 0; k < 4; ++k) o[k] = cvtpk(rbf(v[i][2 * k] - m_bf) * inv, rbf(v[i][2 * k + 1] - m_bf) * inv);
        *(u32x4*)(lds + LN_STAGE + (i ? 8 : wid) * 1024 + lane * 16) = o; }
    BAR();
}

constexpr int GA_LAG = 3, GA_XD = 4;
template <int NT1> struct GaSched {
    static constexpr int NT2 = 6, LAG = GA_LAG, XD = GA_XD, N1 = 18 * NT1, NALL = N1 + 18 * NT2, NST = 6;
    static constexpr int ks_of(int i) { return i < N1 ? i / NT1 : 18 + (i - N1) / NT2; }
    static constexpr int n_of(int i) { return i < N1 ? i % NT1 : (i - N1) % NT2; }
    static constexpr int first_frag(int ks) { return ks < 18 ? ks * NT1 : N1 + (ks - 18) * NT2; }
    static constexpr int sync_step(int st) { return st == 0 ? -1 : st >= NST ? NALL : first_frag(6 * st) - LAG; }
    int sync[NALL] = {}, syncN[NALL] = {}, xw[NALL] = {}, xwN[NALL] = {}, gst[NALL] = {}, gpair[NALL] = {}, xis[NALL] = {}, prio[NALL] = {};
    constexpr GaSched() {
        int posG[NST + 1] = {}, posX[18] = {}, issued = 0;
        for (int i = 0; i < NALL; ++i) { sync[i] = -1; xw[i] = -1; gst[i] = -1; xis[i] = -1; prio[i] = -1; }
        for (int xs = 0; xs < XD; ++xs) { issued += 2; posX[xs] = issued; }
        for (int st = 2; st + 1 < NST; ++st) { const int a = sync_step(st) + 1, len = sync_step(st + 1) - a;
            for (int p = 0; p < 3; ++p) { const int at = a + 4 + (p * (len - 10)) / 3; gst[at] = st + 1; gpair[at] = p; } }
        for (int ks = 0; ks + XD < 18; ++ks) xis[first_frag(ks) + 2] = ks + XD;
        int last = -1;
        for (int i = 0; i < NALL; ++i) {
            for (int st = 1; st < NST; ++st) if (sync_step(st) == i) { sync[i] = st; syncN[i] = st < 3 ? 63 : issued - posG[st]; }
            if (i < N1 && n_of(i) == 0) { xw[i] = ks_of(i); xwN[i] = issued - posX[ks_of(i)]; }
            if (gst[i] >= 0) { issued += 2; posG[gst[i]] = issued; }
            if (xis[i] >= 0) { issued += 2; posX[xis[i]] = issued; }
            if (i == N1 - 1) issued += NT1;
            int st = 0; while (st + 1 < NST && sync_step(st + 1) <= i) ++st;
            const int a = sync_step(st), b = sync_step(st + 1), num = (i - a) * 100 / (b - a);
            const int pr = num < 45 ? 3 : num < 75 ? 2 : num < 92 ? 1 : 0;
            if (pr != last) { prio[i] = pr; last = pr; }
        }
    }
    constexpr bool stores_covered() const { int at = -1; for (int i = 0; i < NALL; ++i) if (gst[i] == 4 && gpair[i] == 2) at = i; return at > N1 - 1; }
};
struct GaRole { int nq, nf1, tq0, tf0; bool isv; };
DI GaRole ga_role(int cg) {
    const int pr = cg >> 1; GaRole r; r.tf0 = pr * 18 + (cg & 1) * 10; r.isv = false;
    if ((cg & 1) == 0) { r.nq = 3; r.nf1 = 4; r.tq0 = pr * 3; }
    else if (pr < 9)   { r.nq = 4; r.nf1 = 2; r.tq0 = 72 + pr * 4; r.isv = true; }
    else if (pr < 12)  { r.nq = 4; r.nf1 = 2; r.tq0 = 48 + (pr - 9) * 4; }
    else               { r.nq = 3; r.nf1 = 2; r.tq0 = 60 + (pr - 12) * 3; }
    return r; }
DI void ga_prefetch(const Params& P, unsigned lds0, int wid, int lane, const GaRole& R) {
    if (wid == 0) return;
    if (wid == 2) glds_in_m(lane * 16, (const char*)P.b1 + R.tf0 * 32, lds0 + GA_BIAS, (1ull << (2 * (R.nf1 + 6))) - 1);
    const char* src[2]; unsigned inc[2]; u64 m[2];
#pragma unroll
    for (int i = 0; i < 2; ++i) { const int ci = 128 * (wid - 1) + 64 * i + lane, row = (ci * 4682) >> 16, c = ci - row * 14, unit = c >> 1, half = c & 1; const bool isq = unit < R.nq;
        const unsigned off = isq ? (unsigned)row * 3456u + (R.tq0 + unit) * 32 + half * 16 : (unsigned)row * 9216u + (R.tf0 + unit - R.nq) * 32 + half * 16;
        src[i] = (isq ? (const char*)P.wqkv : (const char*)P.w1) + off; inc[i] = isq ? 64 * 3456 : 64 * 9216; m[i] = __ballot(unit < R.nq + R.nf1); }
    const unsigned d0 = __builtin_amdgcn_readfirstlane(lds0 + (wid - 1) * 2048);
#pragma unroll
    for (int p = 0; p < 9; ++p) { glds_in_vm(src[0], d0 + p * 64 * GA_ROW, m[0]); glds_in_vm(src[1], d0 + p * 64 * GA_ROW + 1024, m[1]); src[0] += inc[0]; src[1] += inc[1]; }
}

template <int NQ, int NF1, bool ISV, typename Hook>
DI void gemmA_body(const Params& P, char* lds, int rb, int tq0, int tf0, int myflag, u64 tag, Hook& hook) {
    constexpr int NT1 = NQ + NF1, NT2 = 6;
    using SC = GaSched<NT1>; constexpr SC sc{}; constexpr int LAG = SC::LAG, N1 = SC::N1, NALL = SC::NALL, XD = SC::XD;
    static_assert(sc.stores_covered(), "publish point");
    char* ws = P.ws;
    const int tid = threadIdx.x, lane = tid & 63, wid = __builtin_amdgcn_readfirstlane(tid >> 6);
    const unsigned lds0 = (unsigned)(uintptr_t)lds;
    const int T = rb * 8 + wid;
    bf16x8 xf[18][2];
    const char* xbase = uni(ws + XN_OFF + (size_t)(2 * T) * 18 * 1024);
    const unsigned xoff = lane * 16;
    auto x_issue = [&](auto SS) { constexpr int s = SS.value; gload16_ho(xf[s][0], xoff, xbase + s * 1024); gload16_ho(xf[s][1], xoff, xbase + (18 + s) * 1024); };
    static_for<0, XD>([&](auto S) { x_issue(S); });
    unsigned off2[2]; u64 m2[2];
    const unsigned sdst = __builtin_amdgcn_readfirstlane(lds0 + wid * 112 * 16);
    auto g_issue = [&](auto ST, auto PP) { constexpr int st = ST.value, p = PP.value;
        if constexpr (st == 3 && p == 0) {
            int l2 = threadIdx.x & 63; asm volatile("" : "+v"(l2));
#pragma unroll
            for (int i = 0; i < 2; ++i) { const int ci = 112 * wid + 64 * i + l2, row = (ci * 4682) >> 16, c = ci - row * 14;
                off2[i] = (unsigned)row * 9216u + c * 16; m2[i] = __ballot((i == 0 || l2 < 48) && c < 2 * NT2); } }
        const unsigned d = sdst + (st % 3) * GA_SLOT + p * 64 * GA_ROW;
        const char* sb = uni((const char*)P.w1 + (size_t)((st - 3) * 3 + p) * 64 * 9216 + (tf0 + NF1) * 32);
        glds_in_m(off2[0], sb, d, m2[0]); glds_in_m(off2[1], sb, d + 1024, m2[1]); };
    lds_cptr wp0 = (lds_cptr)lds + ((lane >> 4) * 4 + ((lane & 15) >> 2)) * GA_ROW + (lane & 3) * 8, wp1 = wp0 + GA_SLOT, wp2 = wp0 + 2 * GA_SLOT;
    PIN(wp0); PIN(wp1); PIN(wp2);
    const lds_cptr bp = (lds_cptr)lds + GA_BIAS + (lane >> 4) * 8;
    f32x4 acc[7][2];
    bf16x8 wf[LAG + 1];
    auto frag_ptr = [&](auto I) { constexpr int i = I.value, ks = SC::ks_of(i), n = SC::n_of(i), st = ks / 6;
        return (st % 3 == 0 ? wp0 : st % 3 == 1 ? wp1 : wp2) + (ks - 6 * st) * 32 * GA_ROW + n * 32; };
    auto epi_h = [&](int n, int tf) {
#pragma unroll
        for (int u = 0; u < 2; ++u) { u32x4 o;
#pragma unroll
            for (int k = 0; k < 2; ++k) { float a0 = fmaxf(acc[n][u][2 * k], 0.f), a1 = fmaxf(acc[n][u][2 * k + 1], 0.f), b0 = fmaxf(acc[n + 1][u][2 * k], 0.f), b1_ = fmaxf(acc[n + 1][u][2 * k + 1], 0.f);
                o[k] = cvtpk(a0 * a0, a1 * a1); o[2 + k] = cvtpk(b0 * b0, b1_ * b1_); }
            gstore16_ho(xoff, uni(ws + HF_OFF + ((size_t)(2 * T + u) * 144 + (tf >> 1)) * 1024), o); } };
    auto epi_qk = [&](int n, int tq) {
        const float sc_ = tq < 36 ? QSCALE : 1.0f;
        unsigned p0x = cvtpk(acc[n][0][0] * sc_, acc[n][0][1] * sc_), p0y = cvtpk(acc[n][0][2] * sc_, acc[n][0][3] * sc_);
        unsigned p1x = cvtpk(acc[n][1][0] * sc_, acc[n][1][1] * sc_), p1y = cvtpk(acc[n][1][2] * sc_, acc[n][1][3] * sc_);
        auto sx = __builtin_amdgcn_permlane16_swap(p0x, p1x, false, false); auto sy = __builtin_amdgcn_permlane16_swap(p0y, p1y, false, false);
        u32x4 o = {sx[0], sy[0], sx[1], sy[1]};
        gstore16_ho(xoff, uni(ws + QF_OFF + ((size_t)tq * 64 + T) * 1024), o); };
    auto epi_v = [&](int n, int tq) {
        unsigned p0x = cvtpk(acc[n][0][0], acc[n][0][1]), p0y = cvtpk(acc[n][0][2], acc[n][0][3]);
        unsigned p1x = cvtpk(acc[n][1][0], acc[n][1][1]), p1y = cvtpk(acc[n][1][2], acc[n][1][3]);
        auto sx = __builtin_amdgcn_permlane32_swap(p0x, p1x, false, false); auto sy = __builtin_amdgcn_permlane32_swap(p0y, p1y, false, false);
        u32x4 o = {sx[0], sy[0], sx[1], sy[1]};
        const int col = (tq - 72) * 16 + (lane & 15), hd = col / 18, r = col - hd * 18, kg = 2 * T + (lane >> 5), h = (lane >> 4) & 1;
        gstore16_ho((unsigned)((((hd * 128 + kg) * 2 + h) * 18 + r) * 16), uni(ws + VT_OFF), o); };
    static_for<0, LAG>([&](auto I) { wf[I.value] = trfrag(frag_ptr(I), 16 * GA_ROW); });
    static_for<0, NALL>([&](auto I) {
        constexpr int i = I.value, j = i + LAG;
        if constexpr (sc.prio[i] >= 0) __builtin_amdgcn_s_setprio(sc.prio[i]);
        if constexpr (sc.sync[i] >= 0) { SBAR(); wait_vm_bar<sc.syncN[i]>();
            if constexpr (sc.sync[i] == 4) { if (tid == 0) flag_set(ws, FL_QKV, myflag, tag); }
            if constexpr (sc.sync[i] == 5) hook.ask();
            SBAR(); }
        if constexpr (i == SC::sync_step(5) + 12) { SBAR(); hook.check(); SBAR(); }
        if constexpr (i == SC::sync_step(5) + 14) { SBAR(); hook.go(); SBAR(); }
        if constexpr (i > SC::sync_step(5) + 14 && i <= SC::sync_step(5) + 34 && ((i - SC::sync_step(5)) & 1) == 0) { SBAR(); hook.template step<(i - SC::sync_step(5) - 16) / 2>(); SBAR(); }
        if constexpr (j < NALL) wf[j % (LAG + 1)] = trfrag(frag_ptr(IC<j>{}), 16 * GA_ROW);
        constexpr bool p1 = i < N1; constexpr int s = SC::ks_of(i) % 18, n = SC::n_of(i);
        if constexpr (sc.xw[i] >= 0) { wait_vm<sc.xwN[i]>(); PIN(xf[s][0]); PIN(xf[s][1]); }
        const bf16x8 w = wf[i % (LAG + 1)];
        constexpr bool isq = p1 && n < NQ;
        if constexpr (s == 0) {
            if constexpr (isq) { if constexpr (ISV) { mfma16_zero(acc[n][0], xf[s][0], w); mfma16_zero(acc[n][1], xf[s][1], w); } else { mfma16_zero(acc[n][0], w, xf[s][0]); mfma16_zero(acc[n][1], w, xf[s][1]); } }
            else { constexpr int bi = p1 ? n - NQ : NF1 + n; const u32x2 bb = *(const LAS u32x2*)(bp + bi * 32); const f32x4 c0 = {bf_lo(bb[0]), bf_hi(bb[0]), bf_lo(bb[1]), bf_hi(bb[1])};
                mfma16_init(acc[n][0], w, xf[s][0], c0); mfma16_init(acc[n][1], w, xf[s][1], c0); } }
        else if constexpr (isq && ISV) { mfma16_acc(acc[n][0], xf[s][0], w); mfma16_acc(acc[n][1], xf[s][1], w); }
        else { mfma16_acc(acc[n][0], w, xf[s][0]); mfma16_acc(acc[n][1], w, xf[s][1]); }
        SBAR();
        if constexpr (sc.gst[i] >= 0) { g_issue(IC<sc.gst[i]>{}, IC<sc.gpair[i]>{}); SBAR(); }
        if constexpr (sc.xis[i] >= 0) { x_issue(IC<sc.xis[i]>{}); SBAR(); }
        if constexpr (i == N1 - 1) {
            STAMP(5);
            MFMA_DRAIN();
            static_for<0, NT1>([&](auto Q) { PIN(acc[Q.value][0]); PIN(acc[Q.value][1]); });
            static_for<0, NQ>([&](auto Q) { if constexpr (ISV) epi_v(Q.value, tq0 + Q.value); else epi_qk(Q.value, tq0 + Q.value); });
            static_for<0, NF1 / 2>([&](auto Q) { epi_h(NQ + 2 * Q.value, tf0 + 2 * Q.value); });
            STAMP(6);
            SBAR();
        }
    });
    STAMP(7);
    __builtin_amdgcn_s_setprio(0);
    MFMA_DRAIN();
    static_for<0, NT2>([&](auto Q) { PIN(acc[Q.value][0]); PIN(acc[Q.value][1]); });
    static_for<0, NT2 / 2>([&](auto Q) { epi_h(2 * Q.value, tf0 + NF1 + 2 * Q.value); });
}

struct AttnIssue {
    const char* kb0; const char* vb0; unsigned koff, voff, lds0; u64 vmask;
    template <int PART> DI void part(int c, int wid) const { const unsigned d = lds0 + (c % AT_R) * AT_SLOT;
        if constexpr (PART < 2) glds_ho(koff, kb0 + PART * 65536 + c * 8192, d + PART * 8192);
        else glds_ho_m(voff, vb0 + c * 16 * 576 + (PART - 2) * 576, d - wid * 1024 + AT_K + (2 * wid + PART - 2) * AT_VKG, vmask); }
    DI void all(int c, int wid) const { part<0>(c, wid); part<1>(c, wid); part<2>(c, wid); part<3>(c, wid); }
};
DI float half_max(float m) { auto sw = __builtin_amdgcn_permlane32_swap(__float_as_uint(m), __float_as_uint(m), false, false); return fmaxf(__uint_as_float(sw[0]), __uint_as_float(sw[1])); }

template <typename Hook>
DI void attn_fast(const AttnIssue& issue, int wid, lds_cptr kp, lds_cptr vp, const bf16x8 q0, const bf16x8 q1, f32x16& o, bool pre, Hook& hook) {
    if (pre) wait_vm_bar<10>(); else wait_vm_bar<4>();
    f32x16 negm;
    { f32x16 s = MFMA32(ldsfrag(kp), q0, f32x16{}); s = MFMA32(ldsfrag(kp + 8192), q1, s);
      float m = s[0];
#pragma unroll
      for (int i = 1; i < 16; ++i) m = fmaxf(m, s[i]);
      m = half_max(m);
#pragma unroll
      for (int i = 0; i < 16; ++i) negm[i] = -m; }
    PIN(negm);
    f32x16 sa = MFMA32(ldsfrag(kp), q0, negm), sb; sa = MFMA32(ldsfrag(kp + 8192), q1, sa);
    bf16x8 k0 = ldsfrag(kp + 1024), k1 = ldsfrag(kp + 8192 + 1024), v0 = ldsfrag(vp), v1 = v0;
    u32x4 p0 = {0u, 0u, 0u, 0u}, p1 = {0u, 0u, 0u, 0u};
    float t14 = 0.f, t15 = 0.f;
    o = f32x16{};
    PIN(sa); PIN(o); PIN(p0); PIN(p1); PIN(k0); PIN(k1); PIN(v0); PIN(v1);
    MFMA_DRAIN();
#define GRP(wprev, wcur, c0, c1, a0, a1, a2, a3) do { float t0_, t1_, n0_, n1_; \
        asm volatile("v_exp_f32 %2, %6\n\tv_exp_f32 %3, %7\n\tv_cvt_pk_bf16_f32 %0, %10, %11\n\tv_exp_f32 %4, %8\n\tv_exp_f32 %5, %9\n\tv_cvt_pk_bf16_f32 %1, %2, %3" \
            : "=&v"(wprev), "=&v"(wcur), "=&v"(t0_), "=&v"(t1_), "=&v"(n0_), "=&v"(n1_) : "v"(a0), "v"(a1), "v"(a2), "v"(a3), "v"(c0), "v"(c1)); \
        c0 = n0_; c1 = n1_; } while (0)
    lds_cptr kp2 = kp + 2 * AT_SLOT, vp2 = vp + 2 * AT_SLOT;
    PIN(kp); PIN(vp); PIN(kp2); PIN(vp2);
    auto kaddr = [&](auto TT, int blk) { constexpr int t = TT.value, sl = (t >> 3) % AT_R; return (sl < 2 ? kp + sl * AT_SLOT : kp2) + blk * 8192 + (t & 7) * 1024; };
    auto vaddr = [&](auto KG) { constexpr int kg = KG.value, sl = (kg >> 4) % AT_R; return (sl < 2 ? vp + sl * AT_SLOT : vp2) + (kg & 15) * AT_VKG; };
    static_for<0, 64>([&](auto TT) { constexpr int t = TT.value, c = t >> 3, i = t & 7;
        if constexpr (i == 6 && c < 7) { SBAR(); hook.template wait<c>(); hook.template sync<c>(); SBAR(); }
        { constexpr int j = (i + 2) & 7, jp = (j + 7) & 7;
          constexpr int pr = j < 2 ? 3 : j < 5 ? 2 : j < 7 ? 1 : 0, pp = jp < 2 ? 3 : jp < 5 ? 2 : jp < 7 ? 1 : 0;
          if constexpr (pr != pp) { __builtin_amdgcn_s_setprio(pr); SBAR(); } }
        f32x16& sc = (t & 1) ? sb : sa; f32x16& sx = (t & 1) ? sa : sb;
        unsigned wa, wb, wc;
        if constexpr (t + 1 < 64) { sx = MFMA32(k0, q0, negm); SBAR(); }
        if constexpr (t + 2 < 64) { k0 = ldsfrag(kaddr(IC<t + 2>{}, 0)); SBAR(); }
        GRP(wa, wb, t14, t15, sc[0], sc[1], sc[2], sc[3]); p1[3] = wa; p0[0] = wb; SBAR();
        o = MFMA32(v1, __builtin_bit_cast(bf16x8, p1), o); SBAR();
        v1 = ldsfrag(vaddr(IC<2 * t + 1>{})); SBAR();
        GRP(wa, wb, t14, t15, sc[4], sc[5], sc[6], sc[7]); p0[1] = wa; p0[2] = wb; SBAR();
        if constexpr (i >= 6) { if constexpr (c + 2 < 8) { issue.template part<i - 6>(c + 2, wid); SBAR(); } }
        else if constexpr (i < 2) { if constexpr (c >= 1 && c + 1 < 8) { issue.template part<i + 2>(c + 1, wid); SBAR(); } }
        else { hook.template tile<c, i>(); SBAR(); }
        if constexpr (t + 1 < 64) { sx = MFMA32(k1, q1, sx); SBAR(); }
        if constexpr (t + 2 < 64) { k1 = ldsfrag(kaddr(IC<t + 2>{}, 1)); SBAR(); }
        GRP(wa, wc, t14, t15, sc[8], sc[9], sc[10], sc[11]); p0[3] = wa; SBAR();
        o = MFMA32(v0, __builtin_bit_cast(bf16x8, p0), o); SBAR();
        p1[0] = wc;
        if constexpr (t + 1 < 64) { v0 = ldsfrag(vaddr(IC<2 * t + 2>{})); SBAR(); }
        GRP(wa, wb, t14, t15, sc[12], sc[13], sc[14], sc[15]); p1[1] = wa; p1[2] = wb; SBAR();
    });
#undef GRP
    __builtin_amdgcn_s_setprio(0);
    p1[3] = cvtpk(t14, t15);
    o = MFMA32(v1, __builtin_bit_cast(bf16x8, p1), o);
}
DI void attn_safe(const AttnIssue& issue, int wid, lds_cptr kp, lds_cptr vp, const bf16x8 q0, const bf16x8 q1, f32x16& o) {
    issue.all(0, wid); issue.all(1, wid);
    o = f32x16{};
    float m_run = -INFINITY;
    for (int c = 0; c < 8; ++c) {
        if (c == 7) wait_vm_bar<0>(); else wait_vm_bar<4>();
        if (c + 2 < 8) issue.all(c + 2, wid);
        const int so = (c % AT_R) * AT_SLOT;
        for (int i = 0; i < 8; ++i) {
            f32x16 s = MFMA32(ldsfrag(kp + so + i * 1024), q0, f32x16{}); s = MFMA32(ldsfrag(kp + so + 8192 + i * 1024), q1, s);
            float m = s[0];
#pragma unroll
            for (int k = 1; k < 16; ++k) m = fmaxf(m, s[k]);
            m = fmaxf(half_max(m), m_run);
            const float alpha = __builtin_amdgcn_exp2f(m_run - m);
            m_run = m;
#pragma unroll
            for (int k = 0; k < 16; ++k) o[k] *= alpha;
            u32x4 p0, p1;
#pragma unroll
            for (int k = 0; k < 4; ++k) { p0[k] = cvtpk(__builtin_amdgcn_exp2f(s[2 * k] - m), __builtin_amdgcn_exp2f(s[2 * k + 1] - m));
                                          p1[k] = cvtpk(__builtin_amdgcn_exp2f(s[8 + 2 * k] - m), __builtin_amdgcn_exp2f(s[8 + 2 * k + 1] - m)); }
            o = MFMA32(ldsfrag(vp + so + (2 * i) * AT_VKG), __builtin_bit_cast(bf16x8, p0), o);
            o = MFMA32(ldsfrag(vp + so + (2 * i + 1) * AT_VKG), __builtin_bit_cast(bf16x8, p1), o);
        }
    }
}

struct AttnState { AttnIssue issue; u32x4 qa, qb; };
DI void attn_setup(AttnState& st, char* ws, char* lds, int hd) {
    const int tid = threadIdx.x, lane = tid & 63, wid = __builtin_amdgcn_readfirstlane(tid >> 6);
    const int b0 = (hd * 18) >> 4, vr = lane < 21 ? lane : lane - 21;
    st.issue.kb0 = uni(ws + KF_OFF + (size_t)b0 * 65536 + wid * 1024); st.issue.vb0 = uni(ws + VT_OFF + (size_t)hd * 128 * 576 + wid * 2 * 576);
    st.issue.koff = lane * 16; st.issue.voff = (lane < 21 ? 0 : 288) + vr * 16; st.issue.vmask = __ballot(lane < 39 && vr < 18);
    st.issue.lds0 = __builtin_amdgcn_readfirstlane((unsigned)(uintptr_t)lds + wid * 1024);
    if (tid < AT_R * 16 * 2 * 3) { const int i = tid, row = i % 3, hh = (i / 3) & 1, kg = (i / 6) & 15, sl = i / 96;
        const unsigned v = row == 0 ? 0x3f803f80u : 0u;
        *(u32x4*)(lds + sl * AT_SLOT + AT_K + kg * AT_VKG + hh * 336 + (18 + row) * 16) = (u32x4){v, v, v, v}; }
    if (tid == 0) *(volatile int*)(lds + MISC) = 0;
}
template <int N> DI void attn_request(AttnState& st, char* ws, int hd, int qb) {
    const int wid = __builtin_amdgcn_readfirstlane(threadIdx.x >> 6);
    if constexpr (N < 2) { const int b0 = (hd * 18) >> 4, T = qb * 8 + wid;
        gload16_ho(N == 0 ? st.qa : st.qb, st.issue.koff, uni(ws + QF_OFF + ((size_t)(b0 + N) * 64 + T) * 1024)); }
    else st.issue.template part<(N - 2) & 3>((N - 2) >> 2, wid);
}
template <typename Hook, typename Late>
DI void attn_body(AttnState& st, char* ws, char* lds, int hd, int qb, bool pre, Hook& hook, Late& late) {
    const int tid = threadIdx.x, lane = tid & 63, wid = __builtin_amdgcn_readfirstlane(tid >> 6), h = lane >> 5;
    const int off = (hd * 18) & 15, T = qb * 8 + wid;
    if (pre) wait_vm<14>(); else wait_vm<8>();
    PIN(st.qa); PIN(st.qb);
    u32x4 qa = st.qa, qb_ = st.qb;
#pragma unroll
    for (int k = 0; k < 4; ++k) { const int c = 8 * h + 2 * k; if (c < off) qa[k] = 0u; if (c >= off + 2) qb_[k] = 0u; }
    const bf16x8 q0 = __builtin_bit_cast(bf16x8, qa), q1 = __builtin_bit_cast(bf16x8, qb_);
    const int r32 = lane & 31, reff = r32 < 19 ? r32 : ((r32 >= 20 && r32 <= 27) ? 20 : 19);
    const lds_cptr kp = (lds_cptr)lds + lane * 16, vp = (lds_cptr)lds + AT_K + h * 336 + reff * 16;
    f32x16 o;
    STAMP(10);
    attn_fast(st.issue, wid, kp, vp, q0, q1, o, pre, hook);
    STAMP(11);
    { float l = o[10]; auto sw = __builtin_amdgcn_permlane32_swap(__float_as_uint(l), __float_as_uint(l), false, false); l = __uint_as_float(sw[0]);
      float mx = fabsf(o[0]);
#pragma unroll
      for (int k = 1; k < 10; ++k) mx = fmaxf(mx, fabsf(o[k]));
      const bool bad = !(l < 1.0e30f) || !(mx < 1.0e37f) || !(l > 1.0e-30f);
      if (__any(bad) && lane == 0) *(volatile int*)(lds + MISC) = 1; }
    BAR();
    if (*(volatile int*)(lds + MISC)) { BAR(); attn_safe(st.issue, wid, kp, vp, q0, q1, o); BAR(); }
    late();
    float l = o[10]; { auto sw = __builtin_amdgcn_permlane32_swap(__float_as_uint(l), __float_as_uint(l), false, false); l = __uint_as_float(sw[0]); }
    const float inv = 1.0f / l;
    u32x4 ca;
#pragma unroll
    for (int k = 0; k < 4; ++k) ca[k] = cvtpk(o[2 * k] * inv, o[2 * k + 1] * inv);
    char* ob = uni(ws + OF_OFF + ((size_t)hd * 128 + 2 * T) * 512);
    gstore16_ho(((lane >> 4) & 1) * 512 + h * 256 + (lane & 15) * 16, ob, ca);
    char* tb = uni(ws + OT_OFF + (size_t)(2 * T) * 2048 + (hd >> 4) * 1024 + ((hd >> 2) & 3) * 256 + (hd & 3) * 4);
    const unsigned ct = cvtpk(o[8] * inv, o[9] * inv), to = ((lane >> 4) & 1) * 2048 + (lane & 15) * 16;
    asm volatile("s_mov_b64 exec, %3\n\tglobal_store_dword %0, %1, %2 sc1\n\ts_mov_b64 exec, -1" :: "v"(to), "v"(ct), "s"(tb), "s"(0xffffffffull) : "memory");
}

constexpr int CD_PD = 8;
struct CdLoader {
    unsigned voff[5], d0; const char* w2b; const char* wob;
    template <int KIND> DI void offsets(int hw, int lane) {
#pragma unroll
        for (int i = 0; i < 5; ++i) { const int ci = 288 * hw + 64 * i + lane, row = (ci * 3641) >> 16, c = ci - row * 18;
            const int gr = KIND == 0 ? row : KIND == 1 ? 18 * (row >> 4) + (row & 15) : 18 * (row >> 1) + 16 + (row & 1);
            voff[i] = gr * 1152 + c * 16; } }
    DI void init(const Params& P, unsigned lds0, int hw, int lane, int ng, int ks) {
        offsets<0>(hw, lane);
        w2b = uni((const char*)P.w2 + (size_t)ks * 1152 * 1152 + ng * 288); wob = uni((const char*)P.wo + ng * 288);
        d0 = __builtin_amdgcn_readfirstlane(lds0 + CD_RING + hw * 288 * 16); }
    template <int I> DI void one(int t) const {
        const unsigned d = d0 + ((t + 1) % 5) * CD_STAGE; const char* sb = t < 18 ? w2b + (size_t)t * 64 * 1152 : t < 26 ? wob + (size_t)(t - 18) * 72 * 1152 : wob;
        if constexpr (I < 4) glds_in(voff[I], sb, d + I * 1024); else glds_in_m(voff[4], sb, d + 4096, 0xffffffffull); }
    DI void operator()(int t) const { one<0>(t); one<1>(t); one<2>(t); one<3>(t); one<4>(t); }
};
struct HFrags { bf16x8 f[CD_PD][2]; };
template <int S> DI void h_issue(HFrags& H, const char* hb, unsigned hoff) { gload16_ho(H.f[S % CD_PD][0], hoff, hb + S * 1024); gload16_ho(H.f[S % CD_PD][1], hoff, hb + (144 + S) * 1024); }
DI const char* h_base(char* ws, int rb, int ks, int wid) { return uni(ws + HF_OFF + ((size_t)(rb * 8 + 2 * wid) * 144 + ks * 36) * 1024); }

DI void cd_body(const Params& P, char* lds, int rb, int ng, int ks, int oflag, u64 tag, HFrags& H, bool wpre) {
    char* ws = P.ws;
    const int tid = threadIdx.x, lane = tid & 63, wid = __builtin_amdgcn_readfirstlane(tid >> 6);
    const unsigned lds0 = (unsigned)(uintptr_t)lds;
    const int u0 = rb * 8 + 2 * ks;
    CdLoader ld;
    f32x4 acc[9][2];
    if (wid >= 4) {
        const int hw = wid - 4;
        ld.init(P, lds0, hw, lane, ng, ks);
        if (!wpre) { ld(0); ld(1); ld(2); }
        for (int t = 0; t < 18; ++t) {
            wait_vm<10>(); BAR0();
            if (t == 4 && tid == 256) flag_set(ws, FL_O, oflag, tag);
            if (t == 15) ld.offsets<1>(hw, lane);
            ld(t + 3);
            if (t == 9) {
                flag_wait(ws, FL_O, (rb >> 1) * 32 + lane, lane < 32, tag);
                const unsigned oo = (lane >> 5) * 65536 + (lane & 31) * 16;
                const char* ob = uni(ws + OF_OFF + (size_t)u0 * 512 + (size_t)(8 * hw) * 65536);
                const unsigned od = __builtin_amdgcn_readfirstlane(lds0 + CD_O + hw * 8192);
                static_for<0, 8>([&](auto I) { constexpr int i = I.value; glds_ho(oo, ob + (size_t)(i >> 1) * 131072 + (i & 1) * 512, od + i * 1024); });
                glds_ho(lane * 16, uni(ws + OT_OFF + (size_t)(u0 + (hw & 1)) * 2048 + (hw >> 1) * 1024), __builtin_amdgcn_readfirstlane(lds0 + CD_O + 32768 + hw * 1024));
            }
        }
    } else {
        constexpr int LAG = 3, PD = CD_PD, NALL = 36 * 9;
        const char* hb = h_base(ws, rb, ks, wid);
        const unsigned hoff = lane * 16;
        bf16x8 wf[LAG + 1];
        lds_cptr wp0 = (lds_cptr)lds + CD_RING + ((lane >> 4) * 4 + ((lane & 15) >> 2)) * CD_ROW + (lane & 3) * 8, wp2 = wp0 + 2 * CD_STAGE, wp4 = wp0 + 4 * CD_STAGE;
        PIN(wp0); PIN(wp2); PIN(wp4);
        auto frag_ptr = [&](auto I) { constexpr int i = I.value, s = i / 9, n = i % 9, sl = ((s >> 1) + 1) % 5;
            return (sl < 2 ? wp0 : sl < 4 ? wp2 : wp4) + (sl & 1) * CD_STAGE * (sl < 4 ? 1 : 0) + (s & 1) * 32 * CD_ROW + n * 32; };
        BAR0();
        STAMP(14);
        static_for<0, LAG>([&](auto I) { wf[I.value] = trfrag(frag_ptr(I), 16 * CD_ROW); });
        static_for<0, NALL>([&](auto I) {
            constexpr int i = I.value, j = i + LAG, s = i / 9, n = i % 9;
            if constexpr (j < NALL) { if constexpr (j % 18 == 0) { SBAR(); BAR0(); SBAR(); } wf[j % (LAG + 1)] = trfrag(frag_ptr(IC<j>{}), 16 * CD_ROW); }
            if constexpr (n == 0) {
                constexpr int last = s + PD - 2 < 36 ? s + PD - 2 : 35;
                wait_vm<2 * (last - s)>();
                PIN(H.f[s % PD][0]); PIN(H.f[s % PD][1]);
                if constexpr (s + PD - 1 < 36) h_issue<s + PD - 1>(H, hb, hoff);
                SBAR();
            }
            const bf16x8 w = wf[i % (LAG + 1)];
            if constexpr (s == 0) { mfma16_zero(acc[n][0], w, H.f[s % PD][0]); mfma16_zero(acc[n][1], w, H.f[s % PD][1]); }
            else { mfma16_acc(acc[n][0], w, H.f[s % PD][0]); mfma16_acc(acc[n][1], w, H.f[s % PD][1]); }
            SBAR();
        });
        STAMP(15);
        MFMA_DRAIN();
        static_for<0, 9>([&](auto N) { PIN(acc[N.value][0]); PIN(acc[N.value][1]); });
    }
    STAMP(16);
    const int g = lane >> 4;
    char* gpb = ws + GP_OFF + (size_t)(rb * 4 + ng) * 16 * 9 * 1024;
    auto tick_h = [&](auto KK) { constexpr int k = KK.value;
        if constexpr (k <= 6) wait_vm<10>(); else if constexpr (k == 7) wait_vm<5>(); else wait_vm<0>();
        BAR0();
        if constexpr (k == 5) ld.offsets<2>(wid - 4, lane);
        if constexpr (k + 3 < 9) ld(18 + k + 3); };
    if (wid >= 4) {
        const int hw = wid - 4;
        static_for<0, 3>([&](auto K) { tick_h(K); });
        if (tid == 256) flag_set(ws, FL_G, (rb * 4 + ng) * 4 + ks, tag);
        static_for<3, 9>([&](auto K) { tick_h(K); });
        flag_wait(ws, FL_G, (rb * 4 + ng) * 4 + lane, lane < 4, tag);
        u32x4 gp[3][3]; u32x2 bb[3];
        static_for<0, 3>([&](auto UU) { constexpr int un = UU.value;
            if (un < 2 || hw == 0) { const int nt = hw + 4 * un;
                static_for<0, 3>([&](auto K) { constexpr int kk = K.value; const int src = kk + (kk >= ks ? 1 : 0);
                    gload16_ho(gp[un][kk], lane * 16, uni(gpb + ((ks * 4 + src) * 9 + nt) * 1024)); });
                gload8_in(bb[un], (unsigned)((ng * 144 + nt * 16 + g * 4) * 2), P.b2); } });
        wait_vm<0>();
        static_for<0, 3>([&](auto UU) { constexpr int un = UU.value;
            if (un < 2 || hw == 0) { const int nt = hw + 4 * un;
                PIN(gp[un][0]); PIN(gp[un][1]); PIN(gp[un][2]); PIN(bb[un]);
                const float bv[4] = {bf_lo(bb[un][0]), bf_hi(bb[un][0]), bf_lo(bb[un][1]), bf_hi(bb[un][1])};
#pragma unroll
                for (int u = 0; u < 2; ++u) { f32x4* gp_l = (f32x4*)(lds + CD_OWN + (2 * nt + u) * 1024 + lane * 16); f32x4 v = *gp_l;
#pragma unroll
                    for (int k = 0; k < 3; ++k) { v[0] += bf_lo(gp[un][k][2 * u]); v[1] += bf_hi(gp[un][k][2 * u]); v[2] += bf_lo(gp[un][k][2 * u + 1]); v[3] += bf_hi(gp[un][k][2 * u + 1]); }
#pragma unroll
                    for (int i = 0; i < 4; ++i) { const float gg = v[i] + bv[i]; v[i] = gg * __builtin_amdgcn_rcpf(1.0f + __expf(-gg)); }
                    *gp_l = v; } } });
        BAR();
    } else {
        const int qq = (lane & 15) >> 2, p = lane & 3;
        lds_cptr wm = (lds_cptr)lds + CD_RING + (16 * (g >> 1) + 4 * (g & 1) + qq) * CD_ROW + p * 8 + wid * 32, wt = (lds_cptr)lds + CD_RING + (8 * g + qq) * CD_ROW + p * 8 + wid * 32;
        lds_cptr op = (lds_cptr)lds + CD_O + lane * 16;
        lds_cptr wm2 = wm + 2 * CD_STAGE, wm4 = wm + 4 * CD_STAGE;
        PIN(wm); PIN(wm2); PIN(wm4); PIN(wt); PIN(op);
        f32x4 sacc[3][2];
        struct Step { bf16x8 o0, o1, w[3]; };
        constexpr int LA = 2;
        Step stp[LA + 1];
        auto rd = [&](auto KS) { constexpr int s = KS.value, k = s >> 1, sl = (k + 4) % 5;
            Step& st = stp[s % (LA + 1)];
            st.o0 = ldsfrag(op + s * 2048); st.o1 = ldsfrag(op + s * 2048 + 1024);
            static_for<0, 3>([&](auto N) { constexpr int n = N.value;
                const int dn = (n < 2 || wid == 0 ? n : 1) * 128;
                if constexpr (k < 8) { const lds_cptr a = (sl < 2 ? wm : sl < 4 ? wm2 : wm4) + (sl < 4 ? (sl & 1) * CD_STAGE : 0) + (s & 1) * 32 * CD_ROW + dn; st.w[n] = trfrag(a, 8 * CD_ROW); }
                else { const lds_cptr a = wt + sl * CD_STAGE + (s & 1) * 32 * CD_ROW + dn; st.w[n] = trfrag(a, 4 * CD_ROW); } }); };
        auto mm = [&](auto KS) { constexpr int s = KS.value; Step& st = stp[s % (LA + 1)];
            static_for<0, 2>([&](auto N) { constexpr int n = N.value;
                if constexpr (s == 0) { mfma16_zero(sacc[n][0], st.w[n], st.o0); mfma16_zero(sacc[n][1], st.w[n], st.o1); } else { mfma16_acc(sacc[n][0], st.w[n], st.o0); mfma16_acc(sacc[n][1], st.w[n], st.o1); } });
            if (wid == 0) { if constexpr (s == 0) { mfma16_zero(sacc[2][0], st.w[2], st.o0); mfma16_zero(sacc[2][1], st.w[2], st.o1); } else { mfma16_acc(sacc[2][0], st.w[2], st.o0); mfma16_acc(sacc[2][1], st.w[2], st.o1); } } };
        char* gb = uni(gpb + (wid * 4 + ks) * 9 * 1024);
        auto spill = [&](auto NN) { constexpr int n = NN.value;
            if (wid == ks) { *(f32x4*)(lds + CD_OWN + (2 * n) * 1024 + lane * 16) = acc[n][0]; *(f32x4*)(lds + CD_OWN + (2 * n + 1) * 1024 + lane * 16) = acc[n][1]; }
            else gstore16_ho(lane * 16, gb + n * 1024, (u32x4){cvtpk(acc[n][0][0], acc[n][0][1]), cvtpk(acc[n][0][2], acc[n][0][3]), cvtpk(acc[n][1][0], acc[n][1][1]), cvtpk(acc[n][1][2], acc[n][1][3])}); };
        const int col0 = ng * 144 + wid * 16 + g * 4, tok0 = u0 * 16 + (lane & 15);
        u32x2 xx[3][2];
        auto tick = [&](auto KK) { constexpr int k = KK.value; SBAR(); if constexpr (k == 2) wait_vm<0>(); BAR(); STAMP(21 + k);
            if constexpr (k == 4) static_for<0, 3>([&](auto N) { constexpr int n = N.value; if (n < 2 || wid == 0) {
                gload8_in(xx[n][0], (unsigned)((tok0 * DM + col0 + n * 64) * 2), P.x); gload8_in(xx[n][1], (unsigned)(((tok0 + 16) * DM + col0 + n * 64) * 2), P.x); } });
            SBAR(); };
        static_for<0, 9>([&](auto N) { spill(N); });
        tick(IC<0>{});
        static_for<0, LA>([&](auto S) { rd(S); });
        static_for<0, 18>([&](auto S) { constexpr int s = S.value;
            if constexpr (s + LA < 18) { if constexpr (((s + LA) & 1) == 0) tick(IC<(s + LA) / 2>{}); rd(IC<s + LA>{}); }
            SBAR();
            mm(S);
            SBAR();
        });
        STAMP(18);
        MFMA_DRAIN();
        wait_vm<0>();
        static_for<0, 3>([&](auto N) { PIN(sacc[N.value][0]); PIN(sacc[N.value][1]); PIN(xx[N.value][0]); PIN(xx[N.value][1]); });
        BAR();
        STAMP(19);
        static_for<0, 3>([&](auto N) { constexpr int n = N.value; if (n < 2 || wid == 0) {
#pragma unroll
            for (int u = 0; u < 2; ++u) { const int nt = wid + 4 * n; const f32x4 gt = *(const f32x4*)(lds + CD_OWN + (2 * nt + u) * 1024 + lane * 16);
                const float o0 = bf_lo(xx[n][u][0]) + sacc[n][u][0] * gt[0], o1 = bf_hi(xx[n][u][0]) + sacc[n][u][1] * gt[1], o2 = bf_lo(xx[n][u][1]) + sacc[n][u][2] * gt[2], o3 = bf_hi(xx[n][u][1]) + sacc[n][u][3] * gt[3];
                *(u32x2*)(P.out + (size_t)(tok0 + u * 16) * DM + col0 + n * 64) = (u32x2){cvtpk(o0, o1), cvtpk(o2, o3)}; } } });
    }
}

DI int qkv_flag_index(int hd, int qb) {
    int l2 = threadIdx.x & 63; asm volatile("" : "+v"(l2));
    const int b0 = (hd * 18) >> 4, kind = l2 >> 4, i = l2 & 15, rr = kind == 0 ? qb : (i >> 1);
    const int tqk = (kind == 1 ? 36 : 0) + b0 + (i & 1), tv = (hd * 18 + 17 * (i & 1)) >> 4;
    const int cgq = tqk < 48 ? 2 * (tqk / 3) : tqk < 60 ? 2 * (9 + (tqk - 48) / 4) + 1 : 2 * (12 + (tqk - 60) / 3) + 1;
    return rr * 32 + (kind >= 2 ? 2 * (tv >> 2) + 1 : cgq); }
struct HookA { char* ws; char* lds; int wid, lane, hd, qb; u64 tag; AttnState& ast; bool& pre; u32x2 fv;
    DI void ask() { if (wid == 0) { const int i = qkv_flag_index(hd, qb); asm volatile("global_load_dwordx2 %0, %1, %2 sc1" : "=v"(fv) : "v"(i * 8), "s"(ws + FL_QKV) : "memory"); } }
    DI void check() { if (wid == 0) { wait_vm<0>(); PIN(fv); const bool ok = __all(lane >= 48 || (fv[0] == (unsigned)tag && fv[1] == (unsigned)(tag >> 32)));
                                      if (lane == 0) *(volatile int*)(lds + MISC + 16) = ok ? 1 : 0; } }
    DI void go() { BAR(); pre = __builtin_amdgcn_readfirstlane(*(volatile int*)(lds + MISC + 16)) != 0; if (pre) attn_setup(ast, ws, lds, hd); }
    template <int N> DI void step() { if (pre) attn_request<N>(ast, ws, hd, qb); }
};
struct HookB { const Params& P; char* ws; char* lds; unsigned lds0; int tid, lane, wid, j, aflag, rbC, ng, ks; u64 tag; CdLoader ld;
    template <int C> DI void wait() { if constexpr (C >= 3 && C <= 5) { if (wid >= 4) wait_vm<5>(); else wait_vm<0>(); BAR(); } else wait_vm_bar<0>(); }
    template <int C> DI void sync() {
        if constexpr (C == 0) { if (tid == 0) flag_set(ws, FL_H, aflag, tag); }
        if constexpr (C == 1) {
            const int idx = tid; const bool isw2 = idx < 324; const int line = isw2 ? j * 324 + idx : j * 162 + (idx - 324), row = (line * 7282) >> 16, cl = line - row * 9;
            const char* a = (isw2 ? (const char*)P.w2 + (size_t)ks * 1152 * 1152 : (const char*)P.wo) + (size_t)row * 1152 + cl * 128;
            touch(a, lds0 + MISC + 512, __ballot(idx < 486)); }
        if constexpr (C == 2) { if (wid >= 4) ld.init(P, lds0, wid - 4, lane, ng, ks); }
        if constexpr (C == 3) { if (wid == 0) flag_to_lds(lane * 4, ws + FL_H + ((rbC >> 1) * 32 + 8 * ks) * 8, lds0 + MISC + 64, 0xffffull); }
        if constexpr (C == 4) { if (wid == 0) { const unsigned v = *(volatile unsigned*)(lds + MISC + 64 + (lane & 15) * 4);
                                                const bool ok = __all(v == ((lane & 1) ? (unsigned)(tag >> 32) : (unsigned)tag)); if (lane == 0) *(volatile int*)(lds + MISC + 20) = ok ? 1 : 0; } } }
    template <int C, int I> DI void tile() { if constexpr (C >= 3 && C <= 5) { if (wid >= 4) { ld.template one<I - 2>(C - 3); if constexpr (I == 5) ld.template one<4>(C - 3); } } }
};

__global__ __launch_bounds__(512, 2) void fused_block(Params P) {
    extern __shared__ __attribute__((aligned(16))) char lds[];
    char* ws = P.ws;
    const int tid = threadIdx.x, lane = tid & 63, wid = __builtin_amdgcn_readfirstlane(tid >> 6);
    const int b = blockIdx.x, xcd = b & 7, j = b >> 3;
    const unsigned lds0 = (unsigned)(uintptr_t)lds;
    STAMP(0);
    u32x2 xa[2], xb[2]; u64 ep = 0;
    { const char* xr = uni((const char*)P.x + (size_t)b * 8 * 1152);
      const int oc = (lane & 7) + 8 * wid; const unsigned vo = (lane >> 3) * 1152 + (32 * (oc >> 2) + 4 * (oc & 3)) * 2;
      gload8_in(xa[0], vo, xr); gload8_in(xb[0], vo + 32, xr);
      const int oc2 = 64 + (lane & 7); const unsigned vo2 = (lane >> 3) * 1152 + (32 * (oc2 >> 2) + 4 * (oc2 & 3)) * 2;
      gload8_in(xa[1], vo2, xr); gload8_in(xb[1], vo2 + 32, xr); }
    if (wid == 7) {
        u64 pc; asm volatile("s_getpc_b64 %0" : "=s"(pc));
        const char* a = (const char*)(pc & ~127ull) + (size_t)(j * CODE_LINES + lane < 1200 ? j * CODE_LINES + lane : 1200) * 128;
        touch(a, lds0 + MISC + 512, __ballot(lane < CODE_LINES));
    }
    const int rbA = j & 7, cg = xcd * 4 + (j >> 3);
    const GaRole R = ga_role(cg);
    if (wid == 0) {
        ep = __hip_atomic_load((GAS u64*)(ws + FL_OFF), __ATOMIC_RELAXED, __HIP_MEMORY_SCOPE_AGENT);
        wait_vm<0>();
    } else {
        const int idx = 64 * (wid - 1) + lane, row = 72 * rbA + (idx * 10923 >> 16), k = idx - (idx * 10923 >> 16) * 6;
        const int lq = R.nq * 32 - 4, lf = (R.nf1 + 6) * 32 - 4;
        const char* a = k < 2 ? (const char*)P.wqkv + (size_t)row * 3456 + R.tq0 * 32 + (k ? lq : 0)
                              : (const char*)P.w1 + (size_t)row * 9216 + R.tf0 * 32 + (k == 5 ? lf : (k - 2) * 128 < lf ? (k - 2) * 128 : lf);
        touch(a, lds0 + MISC + 512, __ballot(idx < 432));
        wait_vm<1>();
    }
    PIN(xa[0]); PIN(xb[0]); PIN(xa[1]); PIN(xb[1]);
    STAMP(1);
    ln_compute(lds, wid, lane, xa, xb);
    if (wid == 0) {
        const int r = lane >> 3, c = lane & 7, row = b * 8 + r;
        char* dst = uni(ws + XN_OFF + (size_t)(b >> 1) * 18 * 1024);
        static_for<0, 9>([&](auto I) { constexpr int i = I.value; const int oc = c + 8 * i;
            gstore16_ho((oc >> 2) * 1024 + (oc & 3) * 256 + (row & 15) * 16, dst, *(const u32x4*)(lds + LN_STAGE + i * 1024 + lane * 16)); });
    }
    BAR0();
    if (wid == 0) {
        const u64 tag = (((u64)__builtin_amdgcn_readfirstlane((unsigned)(ep >> 32)) << 32) | (unsigned)__builtin_amdgcn_readfirstlane((unsigned)ep)) + 1;
        wait_vm<0>();
        if (lane == 0) { flag_set(ws, FL_LN, b, tag); *(volatile u64*)(lds + LN_PART + 512) = tag; }
        STAMP(2);
        flag_wait(ws, FL_LN, rbA * 32 + lane, lane < 32, tag);
    } else ga_prefetch(P, lds0, wid, lane, R);
    wait_vm_bar<0>();
    STAMP(3);
    const u64 tg = *(volatile u64*)(lds + LN_PART + 512);
    const u64 tag = ((u64)__builtin_amdgcn_readfirstlane((unsigned)(tg >> 32)) << 32) | (unsigned)__builtin_amdgcn_readfirstlane((unsigned)tg);
    const int aflag = rbA * 32 + cg, hd = xcd * 4 + (j & 3), qb = j >> 2;
    AttnState ast; bool pre = false;
    HookA hookA{ws, lds, wid, lane, hd, qb, tag, ast, pre, {0u, 0u}};
    if ((cg & 1) == 0)       gemmA_body<3, 4, false>(P, lds, rbA, R.tq0, R.tf0, aflag, tag, hookA);
    else if ((cg >> 1) < 9)  gemmA_body<4, 2, true>(P, lds, rbA, R.tq0, R.tf0, aflag, tag, hookA);
    else if ((cg >> 1) < 12) gemmA_body<4, 2, false>(P, lds, rbA, R.tq0, R.tf0, aflag, tag, hookA);
    else                     gemmA_body<3, 2, false>(P, lds, rbA, R.tq0, R.tf0, aflag, tag, hookA);
    STAMP(8);
    if (!pre) {
        if (wid == 0) flag_wait(ws, FL_QKV, qkv_flag_index(hd, qb), lane < 48, tag);
        BAR();
        attn_setup(ast, ws, lds, hd);
        static_for<0, 10>([&](auto N) { attn_request<N.value>(ast, ws, hd, qb); });
    }
    if (b == 0 && tid == 0) __hip_atomic_store((GAS u64*)(ws + FL_OFF), tag, __ATOMIC_RELAXED, __HIP_MEMORY_SCOPE_AGENT);
    STAMP(9);
    const int rbC = (xcd >> 2) * 8 + (j & 7), ng = j >> 3, ks = xcd & 3;
    HookB hookB{P, ws, lds, lds0, tid, lane, wid, j, aflag, rbC, ng, ks, tag, {}};
    HFrags H; bool hpre = false;
    auto lateB = [&]() { hpre = __builtin_amdgcn_readfirstlane(*(volatile int*)(lds + MISC + 20)) != 0;
        if (hpre && wid < 4) { const char* hb = h_base(ws, rbC, ks, wid); static_for<0, CD_PD - 1>([&](auto S) { h_issue<S.value>(H, hb, lane * 16); }); } };
    attn_body(ast, ws, lds, hd, qb, pre, hookB, lateB);
    STAMP(12);
    if (!hpre) {
        if (wid == 0) flag_wait(ws, FL_H, (rbC >> 1) * 32 + 8 * ks + lane, lane < 8, tag);
        wait_vm_bar<0>();
        if (wid < 4) { const char* hb = h_base(ws, rbC, ks, wid); static_for<0, CD_PD - 1>([&](auto S) { h_issue<S.value>(H, hb, lane * 16); }); }
    }
    STAMP(13);
    cd_body(P, lds, rbC, ng, ks, qb * 32 + hd, tag, H, true);
    STAMP(20);
}

extern "C" void kernel_launch(void* const* d_in, const int* in_sizes, int n_in, void* d_out, int out_size, void* d_ws, size_t ws_size, hipStream_t stream) {
    Params P;
    P.x = (const u16*)d_in[0]; P.wqkv = (const u16*)d_in[1]; P.wo = (const u16*)d_in[2];
    P.w1 = (const u16*)d_in[4]; P.b1 = (const u16*)d_in[5]; P.w2 = (const u16*)d_in[6]; P.b2 = (const u16*)d_in[7];
    P.ws = (char*)d_ws; P.out = (u16*)d_out;
    (void)hipFuncSetAttribute((const void*)fused_block, hipFuncAttributeMaxDynamicSharedMemorySize, LDS_BYTES);
    fused_block<<<256, 512, LDS_BYTES, stream>>>(P);
}
```

```cpp
#include <hip/hip_runtime.h>
#include <stdint.h>
#include <utility>

#define DI __device__ __forceinline__
#define LAS __attribute__((address_space(3)))
#define GAS __attribute__((address_space(1)))
typedef short bf16x8 __attribute__((ext_vector_type(8)));
typedef short s16x4 __attribute__((ext_vector_type(4)));
typedef float f32x2 __attribute__((ext_vector_type(2)));
typedef float f32x4 __attribute__((ext_vector_type(4)));
typedef float f32x16 __attribute__((ext_vector_type(16)));
typedef unsigned u32x2 __attribute__((ext_vector_type(2)));
typedef unsigned u32x4 __attribute__((ext_vector_type(4)));
typedef __bf16 bf16x2_t __attribute__((ext_vector_type(2)));
typedef unsigned short u16;
typedef unsigned long long u64;

constexpr int DM = 576;
constexpr size_t XN_OFF = 0;
constexpr size_t QF_OFF = XN_OFF + 2359296;
constexpr size_t KF_OFF = QF_OFF + 2359296;
constexpr size_t VT_OFF = KF_OFF + 2359296;
constexpr size_t HF_OFF = VT_OFF + 2359296;
constexpr size_t OF_OFF = HF_OFF + 18874368;
constexpr size_t OT_OFF = OF_OFF + 2097152;
constexpr size_t GP_OFF = OT_OFF + 262144;
constexpr size_t W2T_OFF = GP_OFF + 18874368;
constexpr size_t WOT_OFF = W2T_OFF + 5308416;
constexpr size_t FL_OFF = WOT_OFF + 663552;
constexpr size_t FL_LN = FL_OFF + 4096, FL_QKV = FL_OFF + 8192, FL_H = FL_OFF + 12288, FL_O = FL_OFF + 16384, FL_G = FL_OFF + 20480, FL_W = FL_OFF + 24576;
constexpr size_t PROF_OFF = 128u << 20;
#define STAMP(I) do {} while (0)
#define STAMPA(I) do {} while (0)
#define STAMPD(I) STAMP(I)
DI unsigned cvtpk(float lo, float hi) { f32x2 v = {lo, hi}; bf16x2_t b = __builtin_convertvector(v, bf16x2_t); return __builtin_bit_cast(unsigned, b); }
DI float bf_lo(unsigned w) { return __uint_as_float(w << 16); }
DI float bf_hi(unsigned w) { return __uint_as_float(w & 0xffff0000u); }
DI float rbf(float x) { return bf_lo(cvtpk(x, x)); }
template <int V> struct IC { static constexpr int value = V; };
template <int B, typename Fn, int... Is> DI void static_for_impl(Fn& fn, std::integer_sequence<int, Is...>) { (fn(IC<B + Is>{}), ...); }
template <int B, int E, typename Fn> DI void static_for(Fn&& fn) { if constexpr (B < E) static_for_impl<B>(fn, std::make_integer_sequence<int, E - B>{}); }
#define SBAR() __builtin_amdgcn_sched_barrier(0)
template <typename T> DI void pin_(T& x) { asm volatile("" : "+v"(x)); }
#define PIN(x) pin_(x)
#define MFMA16(a, b, c) __builtin_amdgcn_mfma_f32_16x16x32_bf16((a), (b), (c), 0, 0, 0)
DI void mfma16_acc(f32x4& acc, const bf16x8& a, const bf16x8& b) { asm volatile("v_mfma_f32_16x16x32_bf16 %0, %1, %2, %0" : "+v"(acc) : "v"(a), "v"(b)); }
DI void mfma16_zero(f32x4& acc, const bf16x8& a, const bf16x8& b) { asm volatile("v_mfma_f32_16x16x32_bf16 %0, %1, %2, 0" : "=&v"(acc) : "v"(a), "v"(b)); }
DI void mfma16_init(f32x4& acc, const bf16x8& a, const bf16x8& b, const f32x4& c) { asm volatile("s_nop 1\n\tv_mfma_f32_16x16x32_bf16 %0, %1, %2, %3" : "=&v"(acc) : "v"(a), "v"(b), "v"(c)); }
#define MFMA_DRAIN() asm volatile("s_nop 7\n\ts_nop 7" ::: "memory")
#define MFMA32(a, b, c) __builtin_amdgcn_mfma_f32_32x32x16_bf16((a), (b), (c), 0, 0, 0)

DI void glds_in(unsigned voff, const void* sbase, unsigned lds_base) {
    asm volatile("s_mov_b32 m0, %2\n\ts_nop 0\n\tglobal_load_lds_dwordx4 %0, %1" :: "v"(voff), "s"(sbase), "s"(__builtin_amdgcn_readfirstlane(lds_base)) : "memory"); }
DI void glds_in_m(unsigned voff, const void* sbase, unsigned lds_base, u64 mask) {
    asm volatile("s_mov_b32 m0, %2\n\ts_mov_b64 exec, %3\n\tglobal_load_lds_dwordx4 %0, %1\n\ts_mov_b64 exec, -1" :: "v"(voff), "s"(sbase), "s"(__builtin_amdgcn_readfirstlane(lds_base)), "s"(mask) : "memory"); }
DI void glds_in_vm(const void* g, unsigned lds_base, u64 mask) {
    asm volatile("s_mov_b32 m0, %1\n\ts_mov_b64 exec, %2\n\tglobal_load_lds_dwordx4 %0, off\n\ts_mov_b64 exec, -1" :: "v"(g), "s"(__builtin_amdgcn_readfirstlane(lds_base)), "s"(mask) : "memory"); }
DI void glds_ho(unsigned voff, const void* sbase, unsigned lds_base) {
    asm volatile("s_mov_b32 m0, %2\n\ts_nop 0\n\tglobal_load_lds_dwordx4 %0, %1 sc1" :: "v"(voff), "s"(sbase), "s"(__builtin_amdgcn_readfirstlane(lds_base)) : "memory"); }
DI void glds_ho_m(unsigned voff, const void* sbase, unsigned lds_base, u64 mask) {
    asm volatile("s_mov_b32 m0, %2\n\ts_mov_b64 exec, %3\n\tglobal_load_lds_dwordx4 %0, %1 sc1\n\ts_mov_b64 exec, -1" :: "v"(voff), "s"(sbase), "s"(__builtin_amdgcn_readfirstlane(lds_base)), "s"(mask) : "memory"); }
template <typename T> DI void gload16_ho(T& d, unsigned voff, const void* sbase) { asm volatile("global_load_dwordx4 %0, %1, %2 sc1" : "=v"(d) : "v"(voff), "s"(sbase) : "memory"); }
DI void gload8_ho(u32x2& d, unsigned voff, const void* sbase) { asm volatile("global_load_dwordx2 %0, %1, %2 sc1" : "=v"(d) : "v"(voff), "s"(sbase) : "memory"); }
DI void gload8_in(u32x2& d, unsigned voff, const void* sbase) { asm volatile("global_load_dwordx2 %0, %1, %2" : "=v"(d) : "v"(voff), "s"(sbase) : "memory"); }
DI void gstore16_ho(unsigned voff, void* sbase, u32x4 v) { asm volatile("global_store_dwordx4 %0, %1, %2 sc1\n\ts_nop 1" :: "v"(voff), "v"(v), "s"(sbase) : "memory"); }
template <int N> DI void wait_vm() { asm volatile("s_waitcnt vmcnt(%0)" :: "n"(N) : "memory"); }
template <int N> DI void wait_vm_plus(int e) { if (e == 0) wait_vm<N>(); else if (e == 2) wait_vm<N + 2>(); else if (e == 3) wait_vm<N + 3>(); else wait_vm<N + 4>(); }
template <int N> DI void wait_vm_bar() { asm volatile("s_waitcnt vmcnt(%0) lgkmcnt(0)\n\ts_barrier" :: "n"(N) : "memory"); }
typedef short v4i16_t __attribute__((ext_vector_type(4)));
typedef LAS const char* lds_cptr;
DI s16x4 vtr(lds_cptr p) { return __builtin_bit_cast(s16x4, __builtin_amdgcn_ds_read_tr16_b64_v4i16((LAS v4i16_t*)p)); }
DI bf16x8 trfrag(lds_cptr p, int off2) { s16x4 lo = vtr(p), hi = vtr(p + off2); return __builtin_shufflevector(lo, hi, 0, 1, 2, 3, 4, 5, 6, 7); }
DI bf16x8 ldsfrag(lds_cptr p) { return *(const LAS bf16x8*)p; }
template <typename T> DI T* uni(T* p) {
    uint64_t a = (uint64_t)p; unsigned lo = __builtin_amdgcn_readfirstlane((unsigned)a), hi = __builtin_amdgcn_readfirstlane((unsigned)(a >> 32));
    return (T*)(((uint64_t)hi << 32) | lo); }

DI void flag_set(char* ws, size_t arr, int idx, u64 tag) { __hip_atomic_store((GAS u64*)(ws + arr) + idx, tag, __ATOMIC_RELAXED, __HIP_MEMORY_SCOPE_AGENT); }
DI void flag_wait(char* ws, size_t arr, int idx, bool act, u64 tag) {
    GAS u64* p = (GAS u64*)(ws + arr) + (act ? idx : 0);
    for (unsigned spin = 0; spin < (1u << 19); ++spin) {
        const u64 v = __hip_atomic_load(p, __ATOMIC_RELAXED, __HIP_MEMORY_SCOPE_AGENT);
        if (__all(!act || v == tag)) break;
        __builtin_amdgcn_s_sleep(1);
    }
}
DI void touch(const void* g, unsigned lds_dummy, u64 mask) {
    asm volatile("s_mov_b32 m0, %1\n\ts_mov_b64 exec, %2\n\tglobal_load_lds_dword %0, off\n\ts_mov_b64 exec, -1" :: "v"(g), "s"(__builtin_amdgcn_readfirstlane(lds_dummy)), "s"(mask) : "memory"); }
DI void flag_to_lds(unsigned voff, const void* sbase, unsigned lds_dst, u64 mask) {
    asm volatile("s_mov_b32 m0, %2\n\ts_mov_b64 exec, %3\n\tglobal_load_lds_dword %0, %1 sc1\n\ts_mov_b64 exec, -1" :: "v"(voff), "s"(sbase), "s"(__builtin_amdgcn_readfirstlane(lds_dst)), "s"(mask) : "memory"); }
DI void flag_load(u32x2& d, const char* ws, size_t arr, int idx) { asm volatile("global_load_dwordx2 %0, %1, %2 sc1" : "=v"(d) : "v"(idx * 8), "s"(ws + arr) : "memory"); }
DI bool flag_all(const u32x2& v, bool act, u64 tag) { return __all(!act || (v[0] == (unsigned)tag && v[1] == (unsigned)(tag >> 32))); }
DI int lane_id() { int l = __builtin_amdgcn_mbcnt_hi(~0u, __builtin_amdgcn_mbcnt_lo(~0u, 0u)); asm volatile("" : "+v"(l)); return l; }
#define BAR0() asm volatile("s_barrier" ::: "memory")
#define BAR() asm volatile("s_waitcnt lgkmcnt(0)\n\ts_barrier" ::: "memory")

struct Params { const u16* x; const u16* wqkv; const u16* wo; const u16* w1; const u16* b1; const u16* w2; const u16* b2; char* ws; u16* out; };

constexpr int GA_ROW = 224, GA_SLOT = 192 * GA_ROW, GA_BIAS = 3 * GA_SLOT;
constexpr int LN_PART = GA_BIAS + 1024, LN_STAGE = LN_PART + 1024;
constexpr int AT_K = 8192, AT_VKG = 672, AT_SLOT = AT_K + 8 * AT_VKG, AT_R = 3;
constexpr int F2_ROW = 288, F2_STAGE = 128 * F2_ROW, F2_RING = 40960;
constexpr int JB_OFF = LN_STAGE, OWN_OFF = F2_RING, OI_OFF = 0;
constexpr int MISC = 160000, LDS_BYTES = 163840, TOUCH_DUMMY = MISC + 1024;
constexpr float QSCALE = 0.23570226039551584f * 1.4426950408889634f;

template <int CTRL> DI float dpp_add(float v) { return v + __int_as_float(__builtin_amdgcn_update_dpp(0, __float_as_int(v), CTRL, 0xf, 0xf, true)); }
DI float sum8(float v) { v = dpp_add<0xB1>(v); v = dpp_add<0x4E>(v); return dpp_add<0x141>(v); }
DI void ln_compute(char* lds, int wid, int lane, const u32x2 (&a)[2], const u32x2 (&b)[2]) {
    const int r = lane >> 3, c = lane & 7; const bool two = wid == 1;
    float v[2][8];
#pragma unroll
    for (int i = 0; i < 2; ++i) { v[i][0] = bf_lo(a[i][0]); v[i][1] = bf_hi(a[i][0]); v[i][2] = bf_lo(a[i][1]); v[i][3] = bf_hi(a[i][1]);
                                  v[i][4] = bf_lo(b[i][0]); v[i][5] = bf_hi(b[i][0]); v[i][6] = bf_lo(b[i][1]); v[i][7] = bf_hi(b[i][1]); }
    float* part = (float*)(lds + LN_PART);
    float s = ((v[0][0] + v[0][1]) + (v[0][2] + v[0][3])) + ((v[0][4] + v[0][5]) + (v[0][6] + v[0][7]));
    if (two) s += ((v[1][0] + v[1][1]) + (v[1][2] + v[1][3])) + ((v[1][4] + v[1][5]) + (v[1][6] + v[1][7]));
    s = sum8(s);
    if (c == 0) part[r * 8 + wid] = s;
    BAR();
    const f32x4 p0 = *(const f32x4*)(part + r * 8), p1 = *(const f32x4*)(part + r * 8 + 4);
    const float mean = (((p0[0] + p0[1]) + (p0[2] + p0[3])) + ((p1[0] + p1[1]) + (p1[2] + p1[3]))) * (1.0f / DM);
    float q = 0.f;
#pragma unroll
    for (int k = 0; k < 8; ++k) { const float d = v[0][k] - mean; q += d * d; }
    if (two) {
#pragma unroll
        for (int k = 0; k < 8; ++k) { const float d = v[1][k] - mean; q += d * d; } }
    q = sum8(q);
    if (c == 0) part[64 + r * 8 + wid] = q;
    BAR();
    const f32x4 q0 = *(const f32x4*)(part + 64 + r * 8), q1 = *(const f32x4*)(part + 64 + r * 8 + 4);
    const float var = (((q0[0] + q0[1]) + (q0[2] + q0[3])) + ((q1[0] + q1[1]) + (q1[2] + q1[3]))) * (1.0f / DM);
    const float m_bf = rbf(mean), den = rbf(sqrtf(rbf(rbf(var) + 1e-5f))), inv = 1.0f / den;
#pragma unroll
    for (int i = 0; i < 2; ++i) if (i == 0 || two) { u32x4 o;
#pragma unroll
        for (int k = 0; k < 4; ++k) o[k] = cvtpk(rbf(v[i][2 * k] - m_bf) * inv, rbf(v[i][2 * k + 1] - m_bf) * inv);
        *(u32x4*)(lds + LN_STAGE + (i ? 8 : wid) * 1024 + lane * 16) = o; }
    BAR();
}

#define XA_LAG 3
#define XA_XD 4
constexpr int GA_LAG = XA_LAG, GA_XD = XA_XD;
template <int NQ, int NF1> struct GaSched {
    static constexpr int NT1 = NQ + NF1, NT2 = 6, LAG = GA_LAG, XD = GA_XD, TM = 3, N1 = 18 * NT1, NALL = N1 + 18 * NT2, NST = 6;
    static constexpr int ks_of(int i) { const int nt = i < N1 ? NT1 : NT2, r = i < N1 ? i : i - N1, km = (18 - TM) * nt; return (i < N1 ? 0 : 18) + (r < km ? r / nt : 18 - TM + (r - km) % TM); }
    static constexpr int n_of(int i) { const int nt = i < N1 ? NT1 : NT2, r = i < N1 ? i : i - N1, km = (18 - TM) * nt; return r < km ? r % nt : (r - km) / TM; }
    static constexpr int first_frag(int ks) { return ks < 18 ? ks * NT1 : N1 + (ks - 18) * NT2; }
    static constexpr int sync_step(int st) { return st == 0 ? -1 : st >= NST ? NALL : first_frag(6 * st) - LAG; }
    static constexpr int done_step(int pass, int n) { return (pass ? N1 : 0) + (18 - TM) * (pass ? NT2 : NT1) + TM * n + TM - 1; }
    static constexpr int pub_step() { return sync_step(4) + 22; }
    static constexpr int req_step() { return sync_step(5) + 16; }
    int sync[NALL] = {}, syncN[NALL] = {}, xw[NALL] = {}, xwN[NALL] = {}, gst[NALL] = {}, gpair[NALL] = {}, xis[NALL] = {}, prio[NALL] = {};
    int pubN = 0;
    int cvt[NALL + 1] = {}, cvt2[NALL + 1] = {};
    int st[NALL] = {};
    static constexpr int jt_step() { return first_frag(14) + 3; }
    static constexpr int jr_step(int k) { return sync_step(3) + 1 + 2 * k; }
    static constexpr int jf_step(int j) { return req_step() + 12 + 2 * j; }
    constexpr GaSched() {
        for (int i = 0; i < NALL; ++i) { sync[i] = -1; xw[i] = -1; gst[i] = -1; xis[i] = -1; prio[i] = -1; st[i] = -1; }
        for (int i = 0; i <= NALL; ++i) { cvt[i] = -1; cvt2[i] = -1; }
        auto put = [&](int at, int unit) { if (at > NALL) at = NALL; if (cvt[at] < 0) cvt[at] = unit; else cvt2[at] = unit; };
        for (int q = 0; q < NQ; ++q) put(done_step(0, q) + TM, q);
        for (int k = 0; k < NF1 / 2; ++k) put(done_step(0, NQ + 2 * k + 1) + TM, 16 + k);
        put(done_step(1, 1) + TM, 32); put(done_step(1, 3) + TM, 33); put(NALL, 34);
        { int at = done_step(0, 0) + TM;
          for (int k = 0; k < NT1; ++k) { const int ready = (k < NQ ? done_step(0, k) : done_step(0, NQ + ((k - NQ) | 1))) + TM + 1; at = k == 0 ? ready : at + (k < NQ ? 6 : 8); if (at < ready) at = ready; st[at] = k; } }
        for (int p = 0; p < 3; ++p) { int at = sync_step(1) + 5 + p * 2 * NT1; gst[at] = 3; gpair[at] = p;
                                      at = sync_step(2) + 3 + p * NT1; gst[at] = 4; gpair[at] = p;
                                      at = sync_step(3) + 14 + p * 6; gst[at] = 5; gpair[at] = p; }
        for (int ks = 0; ks + XD < 18; ++ks) xis[first_frag(ks) + 1] = ks + XD;
        int posG[NST + 1] = {}, posX[18] = {}, posQ = 0, issued = 0; bool seen[18] = {};
        for (int xs = 0; xs < XD; ++xs) { issued += 2; posX[xs] = issued; }
        int last = -1;
        for (int i = 0; i < NALL; ++i) {
            for (int s_ = 1; s_ < NST; ++s_) if (sync_step(s_) == i) { sync[i] = s_; syncN[i] = s_ < 3 ? 63 : issued - posG[s_]; }
            if (i == pub_step()) pubN = issued - posQ;
            if (i < N1 && !seen[ks_of(i)]) { seen[ks_of(i)] = true; xw[i] = ks_of(i); xwN[i] = issued - posX[ks_of(i)]; }
            if (gst[i] >= 0) { issued += 2; posG[gst[i]] = issued; }
            if (xis[i] >= 0) { issued += 2; posX[xis[i]] = issued; }
            if (st[i] >= 0) { issued += 1; if (st[i] < NQ) posQ = issued; }
            if (i == jr_step(0) || i == jr_step(1)) issued += 1;
            if (i == jf_step(0) || i == jf_step(1)) issued += 1;
            int s_ = 0; while (s_ + 1 < NST && sync_step(s_ + 1) <= i) ++s_;
            const int a = sync_step(s_), b = sync_step(s_ + 1), num = (i - a) * 100 / (b - a);
            const int pr = num < 45 ? 3 : num < 75 ? 2 : num < 92 ? 1 : 0;
            if (pr != last) { prio[i] = pr; last = pr; }
        }
    }
    constexpr bool order_ok() const {
        int first_st = NALL, last_q = -1, g4 = -1, n = 0;
        for (int i = 0; i < NALL; ++i) { if (st[i] >= 0) { ++n; if (i < first_st) first_st = i; if (st[i] < NQ) last_q = i; } if (gst[i] == 4 && gpair[i] == 2) g4 = i; }
        int g5 = -1; for (int i = 0; i < NALL; ++i) if (gst[i] == 5 && gpair[i] == 0) g5 = i;
        return n == NT1 && g4 <= first_st && last_q < pub_step() && req_step() + 11 < NALL && jf_step(3) < NALL && jr_step(2) < g5; }
};
struct GaRole { int nq, nf1, tq0, tf0; bool isv; };
DI GaRole ga_role(int cg) {
    const int pr = cg >> 1; GaRole r; r.tf0 = pr * 18 + (cg & 1) * 10; r.isv = false;
    if ((cg & 1) == 0) { r.nq = 3; r.nf1 = 4; r.tq0 = pr * 3; }
    else if (pr < 9)   { r.nq = 4; r.nf1 = 2; r.tq0 = 72 + pr * 4; r.isv = true; }
    else if (pr < 12)  { r.nq = 4; r.nf1 = 2; r.tq0 = 48 + (pr - 9) * 4; }
    else               { r.nq = 3; r.nf1 = 2; r.tq0 = 60 + (pr - 12) * 3; }
    return r; }
DI void ga_prefetch(const Params& P, unsigned lds0, int wid, int lane, const GaRole& R) {
    if (wid == 0) return;
    if (wid == 2) glds_in_m(lane * 16, (const char*)P.b1 + R.tf0 * 32, lds0 + GA_BIAS, (1ull << (2 * (R.nf1 + 6))) - 1);
    const char* src[2]; unsigned inc[2]; u64 m[2];
#pragma unroll
    for (int i = 0; i < 2; ++i) { const int ci = 128 * (wid - 1) + 64 * i + lane, row = (ci * 4682) >> 16, c = ci - row * 14, unit = c >> 1, half = c & 1; const bool isq = unit < R.nq;
        const unsigned off = isq ? (unsigned)row * 3456u + (R.tq0 + unit) * 32 + half * 16 : (unsigned)row * 9216u + (R.tf0 + unit - R.nq) * 32 + half * 16;
        src[i] = (isq ? (const char*)P.wqkv : (const char*)P.w1) + off; inc[i] = isq ? 64 * 3456 : 64 * 9216; m[i] = __ballot(unit < R.nq + R.nf1); }
    const unsigned d0 = __builtin_amdgcn_readfirstlane(lds0 + (wid - 1) * 2048);
#pragma unroll
    for (int p = 0; p < 9; ++p) { glds_in_vm(src[0], d0 + p * 64 * GA_ROW, m[0]); glds_in_vm(src[1], d0 + p * 64 * GA_ROW + 1024, m[1]); src[0] += inc[0]; src[1] += inc[1]; }
}

struct HStore { u32x4 v[6]; char* b0; };
struct RetileJob { const Params& P; char* lds; int b, wid;
    unsigned roff, roff8, faddr;
    DI int rank() const { const int q = (b & 63) | ((b >> 7) << 6); return (b & 64) ? q : 128 + q; }
    DI bool third() const { return rank() < 136; }
    DI int be3() const { return 512 + rank(); }
    static DI const char* w2_rows(const Params& P, int be) { const int sl = be / 36; return (const char*)P.w2 + (size_t)((sl >> 2) * 36 + be - sl * 36) * 32 * 1152 + (sl & 3) * 288; }
    struct Blk { int kind, ngb, s; const char* base; };
    DI Blk blk3() const { Blk k; const int be = be3(), q = be - 576; k.ngb = (q * 3641) >> 16; k.s = q - k.ngb * 18; k.kind = be < 576 ? 0 : k.s < 16 ? 1 : 2;
        k.base = k.kind == 0 ? w2_rows(P, be) : (const char*)P.wo + (size_t)(k.kind == 1 ? 36 * k.s : 288 * (k.s - 16)) * 1152 + k.ngb * 288;
        return k; }
    static DI int grow(int kind, int row) { return kind == 0 ? row : kind == 1 ? 18 * (row >> 4) + (row & 15) : 18 * (row >> 1) + 16 + (row & 1); }
    DI void offsets() { const int lane = lane_id();
        const int ci = 64 * wid + lane, r = (ci * 3641) >> 16, c8 = 512 + lane, r8 = (c8 * 3641) >> 16;
        roff = r * 1152 + (ci - r * 18) * 16; roff8 = r8 * 1152 + (c8 - r8 * 18) * 16;
        faddr = JB_OFF + (4 * (lane >> 4) + ((lane & 15) >> 2)) * F2_ROW + (lane & 3) * 8; }
    template <int K> DI void rows() const { const unsigned d = (unsigned)(uintptr_t)lds + JB_OFF + K * 9216;
        if constexpr (K < 2) { const char* sb = uni(w2_rows(P, b + 256 * K)); glds_in(roff, sb, d + wid * 1024); if (wid == K) glds_in(roff8, sb, d + 8192); }
        else if (third()) { const Blk k = blk3(); const char* sb = uni(k.base); const int lane = lane_id();
            auto one = [&](int i) { const int ci = 64 * i + lane, row = (ci * 3641) >> 16, c = ci - row * 18; glds_in(grow(k.kind, row) * 1152 + c * 16, sb, d + i * 1024); };
            one(wid); if (wid == 2) one(8); } }
    DI void put(lds_cptr a, int dr, char* dst) const { const s16x4 lo = vtr(a), hi = vtr(a + dr * F2_ROW);
        gstore16_ho(lane_id() * 16, uni(dst), __builtin_bit_cast(u32x4, __builtin_shufflevector(lo, hi, 0, 1, 2, 3, 4, 5, 6, 7))); }
    DI void frag3(int n) const { const Blk k = blk3(); const int lane = lane_id(), g = lane >> 4, qq = (lane & 15) >> 2;
        const int r1 = k.kind == 0 ? 4 * g + qq : k.kind == 1 ? 16 * (g >> 1) + 4 * (g & 1) + qq : 8 * g + qq;
        put((lds_cptr)lds + JB_OFF + 2 * 9216 + r1 * F2_ROW + (lane & 3) * 8 + n * 32, k.kind == 0 ? 16 : k.kind == 1 ? 8 : 4,
            k.kind == 0 ? P.ws + W2T_OFF + ((size_t)be3() * 9 + n) * 1024 : P.ws + WOT_OFF + ((size_t)(k.ngb * 9 + n) * 18 + k.s) * 1024); }
    DI int stores() const { return 2 + (third() ? 1 : 0) + ((wid < 2 || (wid == 2 && third())) ? 1 : 0); }
    template <int J> DI void frag() const {
        if constexpr (J < 2) put((lds_cptr)lds + faddr + J * 9216 + wid * 32, 16, P.ws + W2T_OFF + ((size_t)(b + 256 * J) * 9 + wid) * 1024);
        else if constexpr (J == 2) { if (third()) frag3(wid); }
        else { if (wid < 2) put((lds_cptr)lds + faddr + wid * 9216 + 8 * 32, 16, P.ws + W2T_OFF + ((size_t)(b + 256 * wid) * 9 + 8) * 1024); else if (wid == 2 && third()) frag3(8); } }
};
template <int NQ, int NF1, bool ISV, typename Hook>
DI void gemmA_body(const Params& P, char* lds, int rb, int tq0, int tf0, int myflag, u64 tag, Hook& hook, HStore& hs, RetileJob& job) {
    constexpr int NT1 = NQ + NF1, NT2 = 6;
    using SC = GaSched<NQ, NF1>; constexpr SC sc{}; constexpr int LAG = SC::LAG, N1 = SC::N1, NALL = SC::NALL, XD = SC::XD;
    static_assert(sc.order_ok(), "publish point");
    char* ws = P.ws;
    const int tid = threadIdx.x, lane = tid & 63, wid = __builtin_amdgcn_readfirstlane(tid >> 6);
    const unsigned lds0 = (unsigned)(uintptr_t)lds;
    const int T = rb * 8 + wid;
    bf16x8 xf[18][2];
    const char* xbase = uni(ws + XN_OFF + (size_t)(2 * T) * 18 * 1024);
    const unsigned xoff = lane * 16;
    auto x_issue = [&](auto SS) { constexpr int s = SS.value; gload16_ho(xf[s][0], xoff, xbase + s * 1024); gload16_ho(xf[s][1], xoff, xbase + (18 + s) * 1024); };
    static_for<0, XD>([&](auto S) { x_issue(S); });
    unsigned off2[2]; u64 m2[2];
    const unsigned sdst = __builtin_amdgcn_readfirstlane(lds0 + wid * 112 * 16);
    auto g_issue = [&](auto ST, auto PP) { constexpr int st = ST.value, p = PP.value;
        if constexpr (st == 3 && p == 0) {
            int l2 = threadIdx.x & 63; asm volatile("" : "+v"(l2));
#pragma unroll
            for (int i = 0; i < 2; ++i) { const int ci = 112 * wid + 64 * i + l2, row = (ci * 4682) >> 16, c = ci - row * 14;
                off2[i] = (unsigned)row * 9216u + c * 16; m2[i] = __ballot((i == 0 || l2 < 48) && c < 2 * NT2); } }
        const unsigned d = sdst + (st % 3) * GA_SLOT + p * 64 * GA_ROW;
        const char* sb = uni((const char*)P.w1 + (size_t)((st - 3) * 3 + p) * 64 * 9216 + (tf0 + NF1) * 32);
        glds_in_m(off2[0], sb, d, m2[0]); glds_in_m(off2[1], sb, d + 1024, m2[1]); };
    lds_cptr wp0 = (lds_cptr)lds + ((lane >> 4) * 4 + ((lane & 15) >> 2)) * GA_ROW + (lane & 3) * 8, wp1 = wp0 + GA_SLOT, wp2 = wp0 + 2 * GA_SLOT;
    PIN(wp0); PIN(wp1); PIN(wp2);
    const lds_cptr bp = (lds_cptr)lds + GA_BIAS + (lane >> 4) * 8;
    f32x4 acc[13][2];
    bf16x8 wf[LAG + 1];
    auto frag_ptr = [&](auto I) { constexpr int i = I.value, ks = SC::ks_of(i), n = SC::n_of(i), st = ks / 6;
        return (st % 3 == 0 ? wp0 : st % 3 == 1 ? wp1 : wp2) + (ks - 6 * st) * 32 * GA_ROW + n * 32; };
    u32x4 pk[7];
    auto cvt_h = [&](int n, u32x4& o0, u32x4& o1) {
#pragma unroll
        for (int u = 0; u < 2; ++u) { u32x4 o;
#pragma unroll
            for (int k = 0; k < 2; ++k) { float a0 = fmaxf(acc[n][u][2 * k], 0.f), a1 = fmaxf(acc[n][u][2 * k + 1], 0.f), b0 = fmaxf(acc[n + 1][u][2 * k], 0.f), b1_ = fmaxf(acc[n + 1][u][2 * k + 1], 0.f);
                o[k] = cvtpk(a0 * a0, a1 * a1); o[2 + k] = cvtpk(b0 * b0, b1_ * b1_); }
            if (u == 0) o0 = o; else o1 = o; } };
    auto cvt_qkv = [&](int n, int tq) {
        const float sc_ = (!ISV && tq < 36) ? QSCALE : 1.0f;
        unsigned p0x = cvtpk(acc[n][0][0] * sc_, acc[n][0][1] * sc_), p0y = cvtpk(acc[n][0][2] * sc_, acc[n][0][3] * sc_);
        unsigned p1x = cvtpk(acc[n][1][0] * sc_, acc[n][1][1] * sc_), p1y = cvtpk(acc[n][1][2] * sc_, acc[n][1][3] * sc_);
        if constexpr (ISV) { auto sx = __builtin_amdgcn_permlane32_swap(p0x, p1x, false, false); auto sy = __builtin_amdgcn_permlane32_swap(p0y, p1y, false, false); pk[n] = (u32x4){sx[0], sy[0], sx[1], sy[1]}; }
        else { auto sx = __builtin_amdgcn_permlane16_swap(p0x, p1x, false, false); auto sy = __builtin_amdgcn_permlane16_swap(p0y, p1y, false, false); pk[n] = (u32x4){sx[0], sy[0], sx[1], sy[1]}; } };
    auto cvt_unit = [&](auto U) { constexpr int u = U.value;
        if constexpr (u < 16) { PIN(acc[u][0]); PIN(acc[u][1]); cvt_qkv(u, tq0 + u); PIN(pk[u]); }
        else if constexpr (u < 32) { constexpr int n = NQ + 2 * (u - 16); PIN(acc[n][0]); PIN(acc[n][1]); PIN(acc[n + 1][0]); PIN(acc[n + 1][1]); cvt_h(n, pk[n], pk[n + 1]); PIN(pk[n]); PIN(pk[n + 1]); }
        else { constexpr int n = 7 + 2 * (u - 32); PIN(acc[n][0]); PIN(acc[n][1]); PIN(acc[n + 1][0]); PIN(acc[n + 1][1]); cvt_h(n, hs.v[n - 7], hs.v[n - 6]); PIN(hs.v[n - 7]); PIN(hs.v[n - 6]); } };
    auto store_unit = [&](auto K) { constexpr int k = K.value;
        if constexpr (k >= NQ) { constexpr int pr = (k - NQ) >> 1, u = (k - NQ) & 1; gstore16_ho(xoff, uni(ws + HF_OFF + ((size_t)(2 * T + u) * 144 + (tf0 >> 1) + pr) * 1024), pk[k]); }
        else if constexpr (ISV) { int l2 = threadIdx.x & 63; asm volatile("" : "+v"(l2));
            const int col = (tq0 + k - 72) * 16 + (l2 & 15), hd = (col * 3641) >> 16, r = col - hd * 18, kg = 2 * T + (l2 >> 5), h = (l2 >> 4) & 1;
            gstore16_ho((unsigned)((((hd * 128 + kg) * 2 + h) * 18 + r) * 16), uni(ws + VT_OFF), pk[k]); }
        else gstore16_ho(xoff, uni(ws + QF_OFF + ((size_t)(tq0 + k) * 64 + T) * 1024), pk[k]); };
    hs.b0 = uni(ws + HF_OFF + ((size_t)(2 * T) * 144 + ((tf0 + NF1) >> 1)) * 1024);
    static_for<0, LAG>([&](auto I) { wf[I.value] = trfrag(frag_ptr(I), 16 * GA_ROW); });
    static_for<0, NALL>([&](auto I) {
        constexpr int i = I.value, j = i + LAG;
        if constexpr (sc.prio[i] >= 0) __builtin_amdgcn_s_setprio(sc.prio[i]);
        if constexpr (sc.sync[i] >= 0) { SBAR();
            STAMPA(22 + 2 * (sc.sync[i] - 1)); wait_vm_bar<sc.syncN[i]>(); STAMPA(23 + 2 * (sc.sync[i] - 1));

            SBAR(); }
        if constexpr (i == SC::pub_step()) { SBAR(); STAMPA(16); wait_vm<sc.pubN>(); BAR0(); STAMPA(17); if (tid == 0) flag_set(ws, FL_QKV, myflag, tag); SBAR(); }
        if constexpr (i == SC::sync_step(4) + 1) { SBAR(); hook.setup(); SBAR(); }
        if constexpr (i == SC::sync_step(5) + 1) { SBAR(); hook.ask(); SBAR(); }
        if constexpr (i == SC::req_step() - 4) { SBAR(); STAMPA(4); hook.check(); SBAR(); }
        if constexpr (i == SC::req_step() - 1) { SBAR(); hook.go(); STAMPA(6); SBAR(); }
        if constexpr (j < NALL) wf[j % (LAG + 1)] = trfrag(frag_ptr(IC<j>{}), 16 * GA_ROW);
        constexpr bool p1 = i < N1; constexpr int s = SC::ks_of(i) % 18, nn = SC::n_of(i), n = p1 ? nn : 7 + nn;
        if constexpr (sc.xw[i] >= 0) { wait_vm<sc.xwN[i]>(); PIN(xf[s][0]); PIN(xf[s][1]); }
        const bf16x8 w = wf[i % (LAG + 1)];
        constexpr bool isq = p1 && nn < NQ;
        if constexpr (s == 0) {
            if constexpr (isq) { if constexpr (ISV) { mfma16_zero(acc[n][0], xf[s][0], w); mfma16_zero(acc[n][1], xf[s][1], w); } else { mfma16_zero(acc[n][0], w, xf[s][0]); mfma16_zero(acc[n][1], w, xf[s][1]); } }
            else { constexpr int bi = p1 ? nn - NQ : NF1 + nn; const u32x2 bb = *(const LAS u32x2*)(bp + bi * 32); const f32x4 c0 = {bf_lo(bb[0]), bf_hi(bb[0]), bf_lo(bb[1]), bf_hi(bb[1])};
                mfma16_init(acc[n][0], w, xf[s][0], c0); mfma16_init(acc[n][1], w, xf[s][1], c0); asm volatile("s_nop 2"); } }
        else if constexpr (isq && ISV) { mfma16_acc(acc[n][0], xf[s][0], w); mfma16_acc(acc[n][1], xf[s][1], w); }
        else { mfma16_acc(acc[n][0], w, xf[s][0]); mfma16_acc(acc[n][1], w, xf[s][1]); }
        SBAR();
        if constexpr (sc.gst[i] >= 0) { g_issue(IC<sc.gst[i]>{}, IC<sc.gpair[i]>{}); SBAR(); }
        if constexpr (sc.xis[i] >= 0) { x_issue(IC<sc.xis[i]>{}); SBAR(); }
        if constexpr (sc.st[i] >= 0) { store_unit(IC<sc.st[i]>{}); SBAR(); }
        if constexpr (i == SC::jt_step()) { job.offsets(); SBAR(); }
        if constexpr (i == SC::jr_step(0)) { job.template rows<0>(); SBAR(); }
        if constexpr (i == SC::jr_step(1)) { job.template rows<1>(); SBAR(); }
        if constexpr (i == SC::jr_step(2)) { job.template rows<2>(); SBAR(); }
        if constexpr (i == SC::jf_step(0)) { job.template frag<0>(); SBAR(); }
        if constexpr (i == SC::jf_step(1)) { job.template frag<1>(); SBAR(); }
        if constexpr (i == SC::jf_step(2)) { job.template frag<2>(); SBAR(); }
        if constexpr (i == SC::jf_step(3)) { job.template frag<3>(); SBAR(); }
        if constexpr (i >= SC::req_step() && i < SC::req_step() + 12 && ((i - SC::req_step()) & 1) == 0) { hook.template step<(i - SC::req_step()) / 2>(); SBAR(); }
        if constexpr (i == SC::req_step() + 11) { STAMPA(21); }
        if constexpr (sc.cvt[i] >= 0) { cvt_unit(IC<sc.cvt[i]>{}); SBAR(); }
        if constexpr (sc.cvt2[i] >= 0) { cvt_unit(IC<sc.cvt2[i]>{}); SBAR(); }
        if constexpr (i == N1 - 1) { STAMP(5); }
    });
    STAMP(7);
    __builtin_amdgcn_s_setprio(0);
    MFMA_DRAIN();
    if constexpr (sc.cvt[NALL] >= 0) cvt_unit(IC<sc.cvt[NALL]>{});
    if constexpr (sc.cvt2[NALL] >= 0) cvt_unit(IC<sc.cvt2[NALL]>{});
}

struct AttnIssue {
    const char* kb0; const char* vb0; unsigned koff, voff, lds0; int wid; u64 vmask;
    template <int PART> DI void part(int c) const { const unsigned so = lds0 + (c % AT_R) * AT_SLOT;
        if constexpr (PART == 0) glds_ho(koff, kb0 + c * 4096, so + wid * 1024); else glds_ho_m(voff, vb0 + c * 8 * 576, so + AT_K + wid * AT_VKG, vmask); }
    DI void all(int c, int) const { part<0>(c); part<1>(c); }
};
DI float half_max(float m) { auto sw = __builtin_amdgcn_permlane32_swap(__float_as_uint(m), __float_as_uint(m), false, false); return fmaxf(__uint_as_float(sw[0]), __uint_as_float(sw[1])); }

struct BcSched {
    static constexpr int LAG = 3, P0 = 6, SPS = 20, NSTEP = 9 * SPS, PL = 15;
    static constexpr int period(int t) { return t < 2 ? -1 : (t - 2) / 4; }
    static constexpr int pos(int t) { return (t + 2) % 4; }
    static constexpr int slot0(int r, int g) { return 5 * r + g; }
    static constexpr int nslot(int g) { return g == 3 ? 2 : 1; }
    static constexpr int st_of(int i) { return i / SPS; }
    static constexpr int kk_of(int i) { return i / SPS < 8 ? (i % SPS) / 5 : i % 4; }
    static constexpr int n_of(int i) { return i / SPS < 8 ? i % 5 : (i % SPS) / 4; }
    static constexpr int req(int t, int g) { const int p = period(t), v = 4 * pos(t) + g;
        if (t < 6 && g == 2) return 16;
        if (p < 0) return 0;
        const int v0 = p + 2 < 16 ? 2 : 0;
        if (v < v0) return 1 + v;
        if (v < v0 + 8) return (p - P0 + 1 >= 0 && p - P0 + 1 < 9) ? 3 + v - v0 : 0;
        if (v < v0 + 13) return (p - P0 + 2 >= 0 && p - P0 + 2 < 9) ? 11 + v - v0 - 8 : 0;
        return 0; }
    int waitN[PL + 1] = {};
    constexpr BcSched() {
        int issued = 2, lastV = 2, lastH = 0;
        for (int t = 0; t < 64; ++t) {
            if (t >= 2 && pos(t) == 0) { const int p = period(t); waitN[p] = p <= 14 ? issued - (lastH > lastV ? lastH : lastV) : 63; }
            for (int g = 0; g < 4; ++g) { const int r = req(t, g); if (r) ++issued; if (r == 2) lastV = issued; if (r == 10) lastH = issued; } } }
};

struct F2State {
    f32x4 acc[5][2]; bf16x8 hf[2][4][2], wf[BcSched::LAG + 1];
    unsigned d0; const char* w2b; const char* hb; lds_cptr wp, wp2; bool five;
    DI void init(const Params& P, char* lds, int wid, int lane, int rb, int ng, int ks) {
        w2b = uni(P.ws + W2T_OFF + (size_t)(ks * 4 + ng) * 36 * 9 * 1024 + wid * 4608);
        d0 = __builtin_amdgcn_readfirstlane((unsigned)(uintptr_t)lds + F2_RING + wid * 4608);
        hb = uni(P.ws + HF_OFF + ((size_t)(rb * 8 + 2 * (wid & 3)) * 144 + ks * 36) * 1024);
        wp = (lds_cptr)lds + F2_RING + lane * 16 + (wid >> 2) * 5 * 1024; wp2 = wp + 2 * F2_STAGE; PIN(wp); PIN(wp2);
        five = wid < 4; }
    template <int ST, int I> DI void w_req(unsigned lane16) const { const unsigned d = d0 + (ST % 3) * F2_STAGE + I * 1024; const char* sb = w2b + (size_t)ST * F2_STAGE + I * 1024;
        if constexpr (I < 4) glds_ho(lane16, sb, d); else glds_ho_m(lane16, sb, d, 0xffffffffull); }
    template <int ST, int KK, int U> DI void h_req(unsigned lane16) { gload16_ho(hf[ST & 1][KK][U], lane16, hb + (U * 144 + 4 * ST + KK) * 1024); }
    template <int I> DI void read() { constexpr int st = BcSched::st_of(I), kk = BcSched::kk_of(I), n = BcSched::n_of(I);
        if (n < 4 || five) wf[I % (BcSched::LAG + 1)] = ldsfrag((st % 3 == 2 ? wp2 : wp + (st % 3) * F2_STAGE) + (kk * 9 + n) * 1024); }
    template <int I> DI void mma() { constexpr int st = BcSched::st_of(I), kk = BcSched::kk_of(I), n = BcSched::n_of(I); const bf16x8 w = wf[I % (BcSched::LAG + 1)];
        if constexpr (n == 0) { PIN(hf[st & 1][kk][0]); PIN(hf[st & 1][kk][1]); }
        if (n < 4 || five) {
            if constexpr (I < 5) { mfma16_zero(acc[n][0], w, hf[st & 1][kk][0]); mfma16_zero(acc[n][1], w, hf[st & 1][kk][1]); }
            else { mfma16_acc(acc[n][0], w, hf[st & 1][kk][0]); mfma16_acc(acc[n][1], w, hf[st & 1][kk][1]); } } }
};

template <typename Hook, typename Fin>
DI void bc_loop(const AttnIssue& issue, F2State& F, lds_cptr kp, lds_cptr vp, const bf16x8 q0, const bf16x8 q1, Hook& hook, Fin& finish) {
    using SC = BcSched; constexpr SC sc{};
    wait_vm_plus<2>(hook.nj); BAR();
    f32x16 negm, o;
    { f32x16 s = MFMA32(ldsfrag(kp), q0, f32x16{}); s = MFMA32(ldsfrag(kp + 4096), q1, s);
      float m = s[0];
#pragma unroll
      for (int i = 1; i < 16; ++i) m = fmaxf(m, s[i]);
      m = half_max(m);
#pragma unroll
      for (int i = 0; i < 16; ++i) negm[i] = -m; }
    PIN(negm);
    f32x16 sa = MFMA32(ldsfrag(kp), q0, negm), sb; sa = MFMA32(ldsfrag(kp + 4096), q1, sa);
    bf16x8 k0 = ldsfrag(kp + 1024), k1 = ldsfrag(kp + 4096 + 1024), v0 = ldsfrag(vp), v1 = v0;
    u32x4 p0 = {0u, 0u, 0u, 0u}, p1 = {0u, 0u, 0u, 0u};
    float t14 = 0.f, t15 = 0.f;
    o = f32x16{};
    PIN(sa); PIN(o); PIN(p0); PIN(p1); PIN(k0); PIN(k1); PIN(v0); PIN(v1); PIN(kp); PIN(vp);
    MFMA_DRAIN();
#define GRP(wprev, wcur, c0, c1, a0, a1, a2, a3) do { float t0_, t1_, n0_, n1_; \
        asm volatile("v_exp_f32 %2, %6\n\tv_exp_f32 %3, %7\n\tv_cvt_pk_bf16_f32 %0, %10, %11\n\tv_exp_f32 %4, %8\n\tv_exp_f32 %5, %9\n\tv_cvt_pk_bf16_f32 %1, %2, %3" \
            : "=&v"(wprev), "=&v"(wcur), "=&v"(t0_), "=&v"(t1_), "=&v"(n0_), "=&v"(n1_) : "v"(a0), "v"(a1), "v"(a2), "v"(a3), "v"(c0), "v"(c1)); \
        c0 = n0_; c1 = n1_; } while (0)
    auto kaddr = [&](auto TT, int blk) { constexpr int t = TT.value; return kp + ((t >> 2) % AT_R) * AT_SLOT + blk * 4096 + (t & 3) * 1024; };
    auto vaddr = [&](auto KG) { constexpr int kg = KG.value; return vp + ((kg >> 3) % AT_R) * AT_SLOT + (kg & 7) * AT_VKG; };
    const unsigned lane16 = issue.koff;
    auto after = [&](auto TT, auto GG) { constexpr int t = TT.value, g = GG.value, p = SC::period(t), r = SC::pos(t);
        if constexpr (p >= SC::P0) {
            static_for<0, SC::nslot(g)>([&](auto J) { constexpr int rd = SC::SPS * (p - SC::P0) + SC::slot0(r, g) + J.value, mi = rd - SC::LAG;
                if constexpr (rd < SC::NSTEP) F.template read<rd>();
                if constexpr (mi >= 0 && mi < SC::NSTEP) F.template mma<mi>();
                if constexpr (mi >= 8 * SC::SPS + 6 && mi < SC::NSTEP && (mi - 8 * SC::SPS - 6) % 4 == 0) hook.template spill<(mi - 8 * SC::SPS - 6) / 4>();
                SBAR(); }); }
        constexpr int rq = SC::req(t, g);
        if constexpr (rq == 1 || rq == 2) issue.template part<rq - 1>(p + 2);
        if constexpr (rq >= 3 && rq <= 10) F.template h_req<p - SC::P0 + 1, (rq - 3) / 2, (rq - 3) % 2>(lane16);
        if constexpr (rq >= 11 && rq <= 15) F.template w_req<p - SC::P0 + 2, rq - 11>(lane16);
        if constexpr (rq == 16) hook.template hstore<t>();
        if constexpr (t == 63 && g == 0) hook.template spill<4>();
        if constexpr (t >= 60) hook.template wo_req<4 * (t - 60) + g>();
        SBAR(); };
    auto barrier = [&](auto PP) { constexpr int p = PP.value;
        SBAR();
        hook.template before<p>(); hook.template wait<p, sc.waitN[p]>();
        hook.template sync<p>(); SBAR(); };
    static_for<0, 64>([&](auto TT) { constexpr int t = TT.value, p = SC::period(t), r = SC::pos(t);
        if constexpr (t >= 2 && r == 0 && p < 15) barrier(IC<p>{});
        { constexpr auto pf = [](int x) { return x == 0 ? 3 : x == 1 ? 2 : x == 2 ? 1 : 0; };
          __builtin_amdgcn_s_setprio(pf(r)); SBAR(); }
        f32x16& scur = (t & 1) ? sb : sa; f32x16& sx = (t & 1) ? sa : sb;
        unsigned wa, wb, wc;
        if constexpr (t + 1 < 64) { sx = MFMA32(k0, q0, negm); SBAR(); }
        if constexpr (t + 2 < 64) { k0 = ldsfrag(kaddr(IC<t + 2>{}, 0)); SBAR(); }
        GRP(wa, wb, t14, t15, scur[0], scur[1], scur[2], scur[3]); p1[3] = wa; p0[0] = wb; SBAR();
        after(TT, IC<0>{});
        o = MFMA32(v1, __builtin_bit_cast(bf16x8, p1), o); SBAR();
        v1 = ldsfrag(vaddr(IC<2 * t + 1>{})); SBAR();
        GRP(wa, wb, t14, t15, scur[4], scur[5], scur[6], scur[7]); p0[1] = wa; p0[2] = wb; SBAR();
        after(TT, IC<1>{});
        if constexpr (t + 1 < 64) { sx = MFMA32(k1, q1, sx); SBAR(); }
        if constexpr (t + 2 < 64) { k1 = ldsfrag(kaddr(IC<t + 2>{}, 1)); SBAR(); }
        GRP(wa, wc, t14, t15, scur[8], scur[9], scur[10], scur[11]); p0[3] = wa; SBAR();
        after(TT, IC<2>{});
        o = MFMA32(v0, __builtin_bit_cast(bf16x8, p0), o); SBAR();
        p1[0] = wc;
        if constexpr (t + 1 < 64) { v0 = ldsfrag(vaddr(IC<2 * t + 2>{})); SBAR(); }
        GRP(wa, wb, t14, t15, scur[12], scur[13], scur[14], scur[15]); p1[1] = wa; p1[2] = wb; SBAR();
        after(TT, IC<3>{});
    });
    __builtin_amdgcn_s_setprio(0);
    p1[3] = cvtpk(t14, t15);
    o = MFMA32(v1, __builtin_bit_cast(bf16x8, p1), o);
    STAMP(11);
    finish(o);
    STAMP(12);
#undef GRP
    __builtin_amdgcn_s_setprio(0);
}
DI void attn_safe(const AttnIssue& issue, int wid, lds_cptr kp, lds_cptr vp, const bf16x8 q0, const bf16x8 q1, f32x16& o) {
    issue.all(0, wid); issue.all(1, wid);
    o = f32x16{};
    float m_run = -INFINITY;
    for (int c = 0; c < 16; ++c) {
        if (c == 15) wait_vm_bar<0>(); else wait_vm_bar<2>();
        if (c + 2 < 16) issue.all(c + 2, wid);
        const int so = (c % AT_R) * AT_SLOT;
        for (int i = 0; i < 4; ++i) {
            f32x16 s = MFMA32(ldsfrag(kp + so + i * 1024), q0, f32x16{}); s = MFMA32(ldsfrag(kp + so + 4096 + i * 1024), q1, s);
            float m = s[0];
#pragma unroll
            for (int k = 1; k < 16; ++k) m = fmaxf(m, s[k]);
            m = fmaxf(half_max(m), m_run);
            const float alpha = __builtin_amdgcn_exp2f(m_run - m);
            m_run = m;
#pragma unroll
            for (int k = 0; k < 16; ++k) o[k] *= alpha;
            u32x4 p0, p1;
#pragma unroll
            for (int k = 0; k < 4; ++k) { p0[k] = cvtpk(__builtin_amdgcn_exp2f(s[2 * k] - m), __builtin_amdgcn_exp2f(s[2 * k + 1] - m));
                                          p1[k] = cvtpk(__builtin_amdgcn_exp2f(s[8 + 2 * k] - m), __builtin_amdgcn_exp2f(s[8 + 2 * k + 1] - m)); }
            o = MFMA32(ldsfrag(vp + so + (2 * i) * AT_VKG), __builtin_bit_cast(bf16x8, p0), o);
            o = MFMA32(ldsfrag(vp + so + (2 * i + 1) * AT_VKG), __builtin_bit_cast(bf16x8, p1), o);
        }
    }
}

struct AttnState { AttnIssue issue; u32x4 qa, qb; };
DI void attn_setup(AttnState& st, char* ws, char* lds, int hd) {
    const int tid = threadIdx.x, lane = tid & 63, wid = __builtin_amdgcn_readfirstlane(tid >> 6);
    const int b0 = (hd * 18) >> 4, vr = lane < 21 ? lane : lane - 21;
    st.issue.kb0 = uni(ws + KF_OFF + (size_t)(b0 + (wid >> 2)) * 65536 + (wid & 3) * 1024); st.issue.vb0 = uni(ws + VT_OFF + (size_t)hd * 128 * 576 + wid * 576);
    st.issue.koff = lane * 16; st.issue.voff = (lane < 21 ? 0 : 288) + vr * 16; st.issue.vmask = __ballot(lane < 39 && vr < 18);
    if (tid < AT_R * 16) { char* p = lds + (tid >> 4) * AT_SLOT + AT_K + ((tid & 15) >> 1) * AT_VKG + (tid & 1) * 336 + 18 * 16;
        *(u32x4*)p = (u32x4){0x3f803f80u, 0x3f803f80u, 0x3f803f80u, 0x3f803f80u}; *(u32x4*)(p + 16) = (u32x4){0u, 0u, 0u, 0u}; *(u32x4*)(p + 32) = (u32x4){0u, 0u, 0u, 0u}; }
    if (tid == 0) { *(volatile int*)(lds + MISC) = 0; *(volatile int*)(lds + MISC + 16) = 0; }
}
template <int N> DI void attn_request(AttnState& st, char* ws, int hd, int qb) {
    const int wid = __builtin_amdgcn_readfirstlane(threadIdx.x >> 6);
    if constexpr (N < 2) { const int b0 = (hd * 18) >> 4, T = qb * 8 + wid;
        gload16_ho(N == 0 ? st.qa : st.qb, st.issue.koff, uni(ws + QF_OFF + ((size_t)(b0 + N) * 64 + T) * 1024)); }
    else st.issue.template part<(N - 2) & 1>((N - 2) >> 1);
}
template <typename Hook>
DI void bc_body(AttnState& st, F2State& F, char* ws, char* lds, int hd, int qb, int wid, Hook& hook) {
    const int lane0 = lane_id(), h0 = lane0 >> 5;
    const int off = (hd * 18) & 15, T = qb * 8 + wid;
    wait_vm_plus<4>(hook.nj);
    PIN(st.qa); PIN(st.qb);
    u32x4 qa = st.qa, qb_ = st.qb;
#pragma unroll
    for (int k = 0; k < 4; ++k) { const int c = 8 * h0 + 2 * k; if (c < off) qa[k] = 0u; if (c >= off + 2) qb_[k] = 0u; }
    const bf16x8 q0 = __builtin_bit_cast(bf16x8, qa), q1 = __builtin_bit_cast(bf16x8, qb_);
    const int r32 = lane0 & 31, reff = r32 < 19 ? r32 : ((r32 >= 20 && r32 <= 27) ? 20 : 19);
    const lds_cptr kp = (lds_cptr)lds + lane0 * 16, vp = (lds_cptr)lds + AT_K + h0 * 336 + reff * 16;
    auto finish = [&](f32x16& o) {
        const int lane = lane_id(), h = lane >> 5;
        { float l = o[10]; auto sw = __builtin_amdgcn_permlane32_swap(__float_as_uint(l), __float_as_uint(l), false, false); l = __uint_as_float(sw[0]);
          float mx = fabsf(o[0]);
#pragma unroll
          for (int k = 1; k < 10; ++k) mx = fmaxf(mx, fabsf(o[k]));
          const bool bad = !(l < 1.0e30f) || !(mx < 1.0e37f) || !(l > 1.0e-30f);
          if (__any(bad) && lane == 0) *(volatile int*)(lds + MISC) = 1; }
        BAR();
        if (*(volatile int*)(lds + MISC)) { BAR(); attn_safe(st.issue, wid, kp, vp, q0, q1, o); BAR(); }
        float l = o[10]; { auto sw = __builtin_amdgcn_permlane32_swap(__float_as_uint(l), __float_as_uint(l), false, false); l = __uint_as_float(sw[0]); }
        const float inv = 1.0f / l;
        u32x4 ca;
#pragma unroll
        for (int k = 0; k < 4; ++k) ca[k] = cvtpk(o[2 * k] * inv, o[2 * k + 1] * inv);
        char* ob = uni(ws + OF_OFF + ((size_t)hd * 128 + 2 * T) * 512);
        gstore16_ho(((lane >> 4) & 1) * 512 + h * 256 + (lane & 15) * 16, ob, ca);
        char* tb = uni(ws + OT_OFF + (size_t)(2 * T) * 2048 + (hd >> 4) * 1024 + ((hd >> 2) & 3) * 256 + (hd & 3) * 4);
        const unsigned ct = cvtpk(o[8] * inv, o[9] * inv), to = ((lane >> 4) & 1) * 2048 + (lane & 15) * 16;
        asm volatile("s_mov_b64 exec, %3\n\tglobal_store_dword %0, %1, %2 sc1\n\ts_mov_b64 exec, -1" :: "v"(to), "v"(ct), "s"(tb), "s"(0xffffffffull) : "memory"); };
    STAMP(10);
    bc_loop(st.issue, F, kp, vp, q0, q1, hook, finish);
}

struct WoFrags { bf16x8 w[18]; };
template <int N> DI void spill_tile(F2State& F, char* ws, char* lds, int wid, int rb, int ng, int ks, unsigned lane16) {
    const int tt = wid & 3, kh = wid >> 2;
    if (N < 4 || kh == 0) { PIN(F.acc[N][0]); PIN(F.acc[N][1]);
        if (tt == ks) { *(f32x4*)(lds + OWN_OFF + (2 * (5 * kh + N)) * 1024 + lane16) = F.acc[N][0]; *(f32x4*)(lds + OWN_OFF + (2 * (5 * kh + N) + 1) * 1024 + lane16) = F.acc[N][1]; }
        else gstore16_ho(lane16, uni(ws + GP_OFF + ((size_t)(rb * 4 + ng) * 16 * 9 + (tt * 4 + ks) * 9 + 5 * kh + N) * 1024),
                         (u32x4){cvtpk(F.acc[N][0][0], F.acc[N][0][1]), cvtpk(F.acc[N][0][2], F.acc[N][0][3]), cvtpk(F.acc[N][1][0], F.acc[N][1][1]), cvtpk(F.acc[N][1][2], F.acc[N][1][3])}); } }

DI void d_body(const Params& P, WoFrags& WO, char* lds, int wid, int rb, int ng, int ks, int oflag, u64 tag) {
    char* ws = P.ws;
    const int lane = lane_id();
    const unsigned lds0 = (unsigned)(uintptr_t)lds, lane16 = lane * 16;
    const int tt = wid & 3, kh = wid >> 2, u0 = rb * 8 + 2 * ks, g = lane >> 4;
    const bool owner = tt == ks;
    char* gpb = ws + GP_OFF + (size_t)(rb * 4 + ng) * 16 * 9 * 1024;
    WoFrags W8;
    { const char* wb = uni(ws + WOT_OFF + (size_t)(ng * 9 + wid) * 18 * 1024); gload16_ho(WO.w[16], lane16, wb + 16 * 1024); gload16_ho(WO.w[17], lane16, wb + 17 * 1024); }
    const int xu = wid & 1, col0 = ng * 144 + g * 4, tok0 = u0 * 16 + (lane & 15);
    u32x4 gp[2][3]; u32x2 xx[3], bb[3];
    wait_vm<2>();
    BAR();
    if (wid == ks && lane == 0) { flag_set(ws, FL_O, oflag, tag); flag_set(ws, FL_G, (rb * 4 + ng) * 4 + ks, tag); }
    STAMP(13);
    auto small = [&]() { static_for<0, 3>([&](auto UU) { constexpr int un = UU.value;
        if (un < 2 || wid < 2) { const int nt = un < 2 ? wid : 8, u = un < 2 ? un : xu;
            gload8_in(xx[un], (unsigned)(((tok0 + u * 16) * DM + col0 + nt * 16) * 2), P.x);
            gload8_in(bb[un], (unsigned)((col0 + nt * 16) * 2), P.b2); } }); };
    if (!owner) small();
    if (owner) { const int oi = (rb >> 1) * 32 + lane;
        if (kh == 0) flag_wait(ws, FL_O, oi, lane < 32 && oi != oflag, tag); else flag_wait(ws, FL_G, (rb * 4 + ng) * 4 + lane, lane < 4 && lane != ks, tag); }
    BAR0();
    STAMP(14);
    static_for<0, 4>([&](auto I) { constexpr int i = I.value;
        glds_ho((lane >> 5) * 65536 + (lane & 31) * 16, uni(ws + OF_OFF + (size_t)u0 * 512 + (size_t)(4 * wid + 2 * (i >> 1)) * 65536 + (i & 1) * 512), lds0 + OI_OFF + wid * 4096 + i * 1024); });
    if (wid < 4) glds_ho(lane16, uni(ws + OT_OFF + (size_t)(u0 + (wid & 1)) * 2048 + (wid >> 1) * 1024), lds0 + OI_OFF + 32768 + wid * 1024);
    static_for<0, 2>([&](auto UU) { constexpr int un = UU.value;
        if (un == 0 || wid < 2) static_for<0, 3>([&](auto K) { constexpr int kk = K.value; const int src = kk + (kk >= ks ? 1 : 0);
            gload16_ho(gp[un][kk], lane16, uni(gpb + ((ks * 4 + src) * 9 + (un ? 8 : wid)) * 1024)); }); });
    if (owner) { small(); if (wid < 2) wait_vm<12>(); else wait_vm<7>(); } else if (wid < 2) wait_vm<6>(); else wait_vm<3>();
    static_for<0, 18>([&](auto S) { PIN(WO.w[S.value]); });
    BAR0();
    STAMP(15);
    if (wid < 2) { const char* wb = uni(ws + WOT_OFF + (size_t)(ng * 9 + 8) * 18 * 1024); static_for<0, 18>([&](auto S) { gload16_ho(W8.w[S.value], lane16, wb + S.value * 1024); }); }
    lds_cptr op = (lds_cptr)lds + OI_OFF + lane16; PIN(op);
    f32x4 sacc[2], saccx;
    bf16x8 o0[3], o1[3];
    static_for<0, 2>([&](auto S) { o0[S.value] = ldsfrag(op + S.value * 2048); o1[S.value] = ldsfrag(op + S.value * 2048 + 1024); });
    static_for<0, 18>([&](auto S) { constexpr int s = S.value;
        if constexpr (s + 2 < 18) { o0[(s + 2) % 3] = ldsfrag(op + (s + 2) * 2048); o1[(s + 2) % 3] = ldsfrag(op + (s + 2) * 2048 + 1024); }
        if constexpr (s == 0) { mfma16_zero(sacc[0], WO.w[s], o0[0]); mfma16_zero(sacc[1], WO.w[s], o1[0]); } else { mfma16_acc(sacc[0], WO.w[s], o0[s % 3]); mfma16_acc(sacc[1], WO.w[s], o1[s % 3]); }
        SBAR(); });
    if (wid < 2) {
        wait_vm<0>(); static_for<0, 18>([&](auto S) { PIN(W8.w[S.value]); });
        lds_cptr ox = op + xu * 1024;
        static_for<0, 2>([&](auto S) { o0[S.value] = ldsfrag(ox + S.value * 2048); });
        static_for<0, 18>([&](auto S) { constexpr int s = S.value;
            if constexpr (s + 2 < 18) o0[(s + 2) % 3] = ldsfrag(ox + (s + 2) * 2048);
            if constexpr (s == 0) mfma16_zero(saccx, W8.w[s], o0[0]); else mfma16_acc(saccx, W8.w[s], o0[s % 3]);
            SBAR(); }); }
    STAMP(18);
    MFMA_DRAIN();
    PIN(sacc[0]); PIN(sacc[1]); PIN(saccx);
    wait_vm<0>();
    static_for<0, 3>([&](auto UU) { constexpr int un = UU.value;
        if (un < 2 || wid < 2) { const int nt = un < 2 ? wid : 8, u = un < 2 ? un : xu;
            constexpr int gi = un < 2 ? 0 : 1;
            PIN(gp[gi][0]); PIN(gp[gi][1]); PIN(gp[gi][2]); PIN(xx[un]); PIN(bb[un]);
            const f32x4 s = un == 0 ? sacc[0] : un == 1 ? sacc[1] : saccx;
            f32x4 v = *(const f32x4*)(lds + OWN_OFF + (2 * nt + u) * 1024 + lane16);
#pragma unroll
            for (int k = 0; k < 3; ++k) { const unsigned g0 = u ? gp[gi][k][2] : gp[gi][k][0], g1 = u ? gp[gi][k][3] : gp[gi][k][1];
                v[0] += bf_lo(g0); v[1] += bf_hi(g0); v[2] += bf_lo(g1); v[3] += bf_hi(g1); }
            const float bv[4] = {bf_lo(bb[un][0]), bf_hi(bb[un][0]), bf_lo(bb[un][1]), bf_hi(bb[un][1])}, xv[4] = {bf_lo(xx[un][0]), bf_hi(xx[un][0]), bf_lo(xx[un][1]), bf_hi(xx[un][1])};
            float o[4];
#pragma unroll
            for (int i = 0; i < 4; ++i) { const float gg = v[i] + bv[i]; o[i] = xv[i] + s[i] * (gg * __builtin_amdgcn_rcpf(1.0f + __expf(-gg))); }
            *(u32x2*)(P.out + (size_t)(tok0 + u * 16) * DM + col0 + nt * 16) = (u32x2){cvtpk(o[0], o[1]), cvtpk(o[2], o[3])}; } });
}

DI int qkv_flag_index(int hd, int qb) {
    int l2 = threadIdx.x & 63; asm volatile("" : "+v"(l2));
    const int b0 = (hd * 18) >> 4, kind = l2 >> 4, i = l2 & 15, rr = kind == 0 ? qb : (i >> 1);
    const int tqk = (kind == 1 ? 36 : 0) + b0 + (i & 1), tv = (hd * 18 + 17 * (i & 1)) >> 4;
    const int cgq = tqk < 48 ? 2 * (tqk / 3) : tqk < 60 ? 2 * (9 + (tqk - 48) / 4) + 1 : 2 * (12 + (tqk - 60) / 3) + 1;
    return rr * 32 + (kind >= 2 ? 2 * (tv >> 2) + 1 : cgq); }
struct HookA { char* ws; char* lds; int wid, hd, qb; u64 tag; AttnState& ast; bool& pre; u32x2 fv;
    DI void setup() { attn_setup(ast, ws, lds, hd); }
    DI void ask() { if (wid == 7) { const int i = qkv_flag_index(hd, qb); asm volatile("global_load_dwordx2 %0, %1, %2 sc1" : "=v"(fv) : "v"(i * 8), "s"(ws + FL_QKV) : "memory"); } }
    DI void check() { if (wid == 7) { const int lane = lane_id(); wait_vm<0>(); PIN(fv); const bool ok = __all(lane >= 48 || (fv[0] == (unsigned)tag && fv[1] == (unsigned)(tag >> 32)));
                                      if (ok && lane == 0) *(volatile int*)(lds + MISC + 16) = 1; } }
    DI void go() { pre = __builtin_amdgcn_readfirstlane(*(volatile int*)(lds + MISC + 16)) != 0; }
    template <int N> DI void step() { if (pre) attn_request<N>(ast, ws, hd, qb); }
};
struct HookB { const Params& P; char* ws; char* lds; unsigned lds0; int wid, aflag, rbC, ng, ks; u64 tag; HStore& hs; F2State& F; WoFrags& WO; unsigned lane16; int nj;
    template <int S> DI void wo_req() { gload16_ho(WO.w[S], lane16, uni(ws + WOT_OFF + ((size_t)(ng * 9 + wid) * 18 + S) * 1024)); }
    template <int T> DI void hstore() { gstore16_ho(lane16, hs.b0 + ((T & 1) * 144 + (T >> 1)) * 1024, hs.v[T]); }
    template <int N> DI void spill() { spill_tile<N>(F, ws, lds, wid, rbC, ng, ks, lane16); }
    DI int pre_flag(int lane) const { const int be = lane < 36 ? (ks * 4 + ng) * 36 + lane : 576 + ng * 18 + lane - 36;
        const int rk = be - 512, q = rk & 127, bp = be < 512 ? be & 255 : rk < 128 ? ((q & 63) | 64 | ((q >> 6) << 7)) : ((q & 63) | ((q >> 6) << 7));
        return lane < 54 ? ((bp >> 3) & 7) * 32 + (bp & 7) * 4 + (bp >> 6) : (rbC >> 1) * 32 + 8 * ks + (lane < 62 ? lane - 54 : 0); }
    template <int PP, int N> DI void wait() { if constexpr (PP == 0) { wait_vm_plus<N>(nj); BAR(); } else wait_vm_bar<N>(); }
    template <int PP> DI void before() {
        if constexpr (PP == BcSched::P0 - 2) { if (wid == 0) { const int lane = lane_id();
            wait_vm<2>(); const unsigned lo = *(volatile unsigned*)(lds + MISC + 384 + lane * 4), hi = *(volatile unsigned*)(lds + MISC + 640 + lane * 4);
            if (!__all(lane >= 62 || (lo == (unsigned)tag && hi == (unsigned)(tag >> 32)))) flag_wait(ws, FL_H, pre_flag(lane), lane < 62, tag); } } }
    template <int PP> DI void sync() {
        if constexpr (PP == 2) { if (wid == 0 && lane16 == 0) flag_set(ws, FL_H, aflag, tag);
                                 F.init(P, lds, wid, lane_id(), rbC, ng, ks); }
        if constexpr (PP == BcSched::P0 - 3) { if (wid == 0) { const unsigned o = pre_flag(lane_id()) * 8;
            flag_to_lds(o, ws + FL_H, lds0 + MISC + 384, (1ull << 62) - 1); flag_to_lds(o + 4, ws + FL_H, lds0 + MISC + 640, (1ull << 62) - 1); } } }
};

__global__ __launch_bounds__(512, 2) void fused_block(Params P) {
    extern __shared__ __attribute__((aligned(16))) char lds[];
    char* ws = P.ws;
    const int tid = threadIdx.x, lane = tid & 63, wid = __builtin_amdgcn_readfirstlane(tid >> 6);
    const int b = blockIdx.x, xcd = b & 7, j = b >> 3;
    const unsigned lds0 = (unsigned)(uintptr_t)lds;
    STAMP(0);
    u32x2 xa[2], xb[2]; u64 ep = 0;
    { const char* xr = uni((const char*)P.x + (size_t)b * 8 * 1152);
      const int oc = (lane & 7) + 8 * wid; const unsigned vo = (lane >> 3) * 1152 + (32 * (oc >> 2) + 4 * (oc & 3)) * 2;
      gload8_in(xa[0], vo, xr); gload8_in(xb[0], vo + 32, xr);
      const int oc2 = 64 + (lane & 7); const unsigned vo2 = (lane >> 3) * 1152 + (32 * (oc2 >> 2) + 4 * (oc2 & 3)) * 2;
      gload8_in(xa[1], vo2, xr); gload8_in(xb[1], vo2 + 32, xr); }
    if (wid == 7) {
        u64 pc; int len;
        asm volatile("s_getpc_b64 %0\n.Lfb_code_here:\n\ts_mov_b32 %1, .Lfb_code_end-.Lfb_code_here" : "=s"(pc), "=s"(len));
        const int nl = len > 0 ? len >> 7 : 0, per = (nl + 31) >> 5, line = j * per + lane;
        const bool in = lane < per && line < nl;
        touch((const char*)(pc & ~127ull) + (size_t)(in ? line : 0) * 128, lds0 + TOUCH_DUMMY, __ballot(in) | 1ull);
    }
    const int rbA = j & 7, cg = xcd * 4 + (j >> 3);
    const GaRole R = ga_role(cg);
    if (wid == 0) {
        ep = __hip_atomic_load((GAS u64*)(ws + FL_OFF), __ATOMIC_RELAXED, __HIP_MEMORY_SCOPE_AGENT);
        wait_vm<0>();
    }
    else if (wid == 7) wait_vm<1>();
    else wait_vm<0>();
    PIN(xa[0]); PIN(xb[0]); PIN(xa[1]); PIN(xb[1]);
    STAMP(1);
    if (wid != 0) {
        const int idx = 64 * (wid - 1) + lane, row = 72 * rbA + (idx * 10923 >> 16), k = idx - (idx * 10923 >> 16) * 6;
        const int lq = R.nq * 32 - 4, lf = (R.nf1 + 6) * 32 - 4;
        const char* a = k < 2 ? (const char*)P.wqkv + (size_t)row * 3456 + R.tq0 * 32 + (k ? lq : 0)
                              : (const char*)P.w1 + (size_t)row * 9216 + R.tf0 * 32 + (k == 5 ? lf : (k - 2) * 128 < lf ? (k - 2) * 128 : lf);
        touch(a, lds0 + TOUCH_DUMMY, __ballot(idx < 432));
    }
    ln_compute(lds, wid, lane, xa, xb);
    if (wid == 0) {
        const int r = lane >> 3, c = lane & 7, row = b * 8 + r;
        char* dst = uni(ws + XN_OFF + (size_t)(b >> 1) * 18 * 1024);
        static_for<0, 9>([&](auto I) { constexpr int i = I.value; const int oc = c + 8 * i;
            gstore16_ho((oc >> 2) * 1024 + (oc & 3) * 256 + (row & 15) * 16, dst, *(const u32x4*)(lds + LN_STAGE + i * 1024 + lane * 16)); });
    }
    BAR0();
    if (wid == 0) {
        const u64 tag = (((u64)__builtin_amdgcn_readfirstlane((unsigned)(ep >> 32)) << 32) | (unsigned)__builtin_amdgcn_readfirstlane((unsigned)ep)) + 1;
        wait_vm<0>();
        if (lane == 0) { flag_set(ws, FL_LN, b, tag); *(volatile u64*)(lds + LN_PART + 512) = tag; }
        STAMP(2);
        flag_wait(ws, FL_LN, rbA * 32 + lane, lane < 32, tag);
    } else ga_prefetch(P, lds0, wid, lane, R);
    wait_vm_bar<0>();
    STAMP(3);
    const u64 tg = *(volatile u64*)(lds + LN_PART + 512);
    const u64 tag = ((u64)__builtin_amdgcn_readfirstlane((unsigned)(tg >> 32)) << 32) | (unsigned)__builtin_amdgcn_readfirstlane((unsigned)tg);
    const int aflag = rbA * 32 + cg, hd = xcd * 4 + (j & 3), qb = j >> 2;
    AttnState ast; bool pre = false; HStore hs;
    ast.issue.lds0 = lds0; ast.issue.wid = wid;
    HookA hookA{ws, lds, wid, hd, qb, tag, ast, pre, {0u, 0u}};
    RetileJob job{P, lds, b, wid, 0u, 0u, 0u};
    if ((cg & 1) == 0)       gemmA_body<3, 4, false>(P, lds, rbA, R.tq0, R.tf0, aflag, tag, hookA, hs, job);
    else if ((cg >> 1) < 9)  gemmA_body<4, 2, true>(P, lds, rbA, R.tq0, R.tf0, aflag, tag, hookA, hs, job);
    else if ((cg >> 1) < 12) gemmA_body<4, 2, false>(P, lds, rbA, R.tq0, R.tf0, aflag, tag, hookA, hs, job);
    else                     gemmA_body<3, 2, false>(P, lds, rbA, R.tq0, R.tf0, aflag, tag, hookA, hs, job);
    STAMP(8);
    if (!pre) {
        flag_wait(ws, FL_QKV, qkv_flag_index(hd, qb), lane_id() < 48, tag);
        static_for<0, 6>([&](auto N) { attn_request<N.value>(ast, ws, hd, qb); });
    }
    if (b == 0 && tid == 0) __hip_atomic_store((GAS u64*)(ws + FL_OFF), tag, __ATOMIC_RELAXED, __HIP_MEMORY_SCOPE_AGENT);
    STAMP(9);
    const int rbC = (xcd >> 2) * 8 + (j & 7), ng = j >> 3, ks = xcd & 3;
    F2State F;
    ast.issue.lds0 = lds0; ast.issue.wid = wid;
    WoFrags WO;
    HookB hookB{P, ws, lds, lds0, wid, aflag, rbC, ng, ks, tag, hs, F, WO, ast.issue.koff, pre ? job.stores() : 0};
    bc_body(ast, F, ws, lds, hd, qb, wid, hookB);
    d_body(P, WO, lds, wid, rbC, ng, ks, qb * 32 + hd, tag);
    STAMP(20);
    asm volatile(".Lfb_code_end:");
}

extern "C" void kernel_launch(void* const* d_in, const int* in_sizes, int n_in, void* d_out, int out_size, void* d_ws, size_t ws_size, hipStream_t stream) {
    Params P;
    P.x = (const u16*)d_in[0]; P.wqkv = (const u16*)d_in[1]; P.wo = (const u16*)d_in[2];
    P.w1 = (const u16*)d_in[4]; P.b1 = (const u16*)d_in[5]; P.w2 = (const u16*)d_in[6]; P.b2 = (const u16*)d_in[7];
    P.ws = (char*)d_ws; P.out = (u16*)d_out;
    (void)hipFuncSetAttribute((const void*)fused_block, hipFuncAttributeMaxDynamicSharedMemorySize, LDS_BYTES);
    fused_block<<<256, 512, LDS_BYTES, stream>>>(P);
}
```
